```python
import torch

def _softmax(x, axis=-1):
    return torch.softmax(x, dim=axis)
def _gelu(x):
    return torch.nn.functional.gelu(x, approximate="tanh")
def _silu(x):
    return torch.nn.functional.silu(x)
def _sigmoid(x):
    return torch.sigmoid(x)
def _layernorm(x, axis=-1, eps=1e-5):
    m = x.mean(dim=axis, keepdim=True)
    v = x.var(dim=axis, keepdim=True, unbiased=False)
    return (x - m) / torch.sqrt(v + eps)
def _scatter_add(tgt, idx, src, axis=0):
    return tgt.clone().index_add_(axis, idx, src)
def _segment_sum(src, seg, n):
    out = torch.zeros((n,) + src.shape[1:], dtype=src.dtype)
    return out.index_add_(0, seg, src)
def _pad(x, pw):
    flat = tuple(v for pair in reversed(pw) for v in pair)
    return torch.nn.functional.pad(x, flat)

kernel_name = 'gated_mix__ssm_conv_h3__glu_gated_bilinear__prompt'

def setup_inputs():

    _w = (lambda a: (lambda t: t.to(torch.bfloat16) if t.is_floating_point() else t)(torch.as_tensor(a)))
    def _A_setup():
        B, S, D = 1, 2048, 640
        ks = 4
        return {
            "q": _w((lambda *s: torch.randn(*s, dtype=torch.bfloat16) * 0.05)(B, S, D)),
            "k": _w((lambda *s: torch.randn(*s, dtype=torch.bfloat16) * 0.05)(B, S, D)),
            "v": _w((lambda *s: torch.randn(*s, dtype=torch.bfloat16) * 0.05)(B, S, D)),
            "shift_k": _w((lambda *s: torch.randn(*s, dtype=torch.bfloat16) * 0.05)(D, ks) * 0.5),
            "ssm_k": _w((lambda *s: torch.randn(*s, dtype=torch.bfloat16) * 0.1)(D, S) * (1.0 / S ** 0.5)),
            "ssm_D": _w((lambda *s: torch.randn(*s, dtype=torch.bfloat16) * 0.05)(D)),
        }
    _A_in = _A_setup()

    def _B_setup():
        B, S, D, F = 1, 2048, 640, 1536
        return {
            "x": _w((lambda *s: torch.randn(*s, dtype=torch.bfloat16) * 0.05)(B, S, D)),
            "w_gate": _w((lambda *s: torch.randn(*s, dtype=torch.bfloat16) * 0.1)(D, F) * (D ** -0.5)),
            "w_up": _w((lambda *s: torch.randn(*s, dtype=torch.bfloat16) * 0.1)(D, F) * (D ** -0.5)),
            "w_down": _w((lambda *s: torch.randn(*s, dtype=torch.bfloat16) * 0.1)(F, D) * (F ** -0.5)),
        }
    _B_in = _B_setup()

    return {
        "A_q": _A_in["q"],
        "A_k": _A_in["k"],
        "A_v": _A_in["v"],
        "A_shift_k": _A_in["shift_k"],
        "A_ssm_k": _A_in["ssm_k"],
        "A_ssm_D": _A_in["ssm_D"],
        "B_x": _B_in["x"],
        "B_w_gate": _B_in["w_gate"],
        "B_w_up": _B_in["w_up"],
        "B_w_down": _B_in["w_down"],
        "gate_w": _w((lambda *s: torch.randn(*s, dtype=torch.bfloat16) * 0.05)(640) * 16.0),
    }

def reference(A_q, A_k, A_v, A_shift_k, A_ssm_k, A_ssm_D, B_x, B_w_gate, B_w_up, B_w_down, gate_w):
    def _A_ref(q, k, v, shift_k, ssm_k, ssm_D):
        B, S, D = q.shape
        ks = shift_k.shape[-1]
        k_pad = _pad(k, ((0, 0), (ks - 1, 0), (0, 0)))
        k_shift = torch.zeros_like(k)
        for t in range(ks):
            k_shift = k_shift + k_pad[:, t:t + S, :] * shift_k[:, ks - 1 - t][None, None, :]
        kv = k_shift * v
        L = 2 * S
        kv_pad = _pad(kv, ((0, 0), (0, S), (0, 0)))
        kf_pad = _pad(ssm_k, ((0, 0), (0, S)))
        kv_f = (lambda x, axes=(-2,-1): torch.fft.rfft2(x.float(), dim=axes))(kv_pad[:, None, :, :], axes=(1, 2))
        kf_f = (lambda x, axes=(-2,-1): torch.fft.rfft2(x.float(), dim=axes))(kf_pad[:, None, :], axes=(1, 2))
        kf_f = (lambda x, *d: x.permute(*d))(kf_f, 1, 2, 0)
        y_f = kv_f * kf_f[None, :, :, :]
        y = (lambda x, s, axes=(-2,-1): torch.fft.irfft2(x, s=s, dim=axes).to(torch.bfloat16))(y_f, (1, L), axes=(1, 2))[:, 0, :S, :]
        y = y + kv * ssm_D[None, None, :]
        return q * y

    def _B_ref(x, w_gate, w_up, w_down):
        g = torch.einsum("bsd,df->bsf", x, w_gate)
        u = torch.einsum("bsd,df->bsf", x, w_up)
        h = g * u
        return torch.einsum("bsf,fd->bsd", h, w_down)

    x = A_q
    g = _sigmoid((x * gate_w).sum(-1, keepdims=True))
    a = _A_ref(x, A_k, A_v, A_shift_k, A_ssm_k, A_ssm_D)
    b = _B_ref(x, B_w_gate, B_w_up, B_w_down)
    return g * a + (1 - g) * b
```

```cpp
#include <hip/hip_runtime.h>
#include <hip/hip_bf16.h>
typedef unsigned short u16;
__device__ __forceinline__ float b2f(u16 x){ return __uint_as_float(((unsigned)x)<<16); }
__device__ __forceinline__ u16 f2b(float f){ unsigned u=__float_as_uint(f); return (u16)((u + 0x7FFF + ((u>>16)&1))>>16); }
__device__ __forceinline__ float rb(float f){ return b2f(f2b(f)); }
constexpr int S=2048, D=640, F=1536;
__global__ void k_kv(const u16* k,const u16* v,const u16* sk,u16* kv){
  int i=blockIdx.x*256+threadIdx.x; int s=i/D,d=i%D;
  float acc=0.f;
  for(int t=0;t<4;++t){ int ss=s+t-3; float kk= ss>=0? b2f(k[ss*D+d]):0.f; acc=rb(acc+rb(kk*b2f(sk[d*4+3-t]))); }
  kv[i]=f2b(acc*b2f(v[i]));
}
__global__ void k_conv(const u16* kv,const u16* h,const u16* Dd,const u16* q,u16* a){
  int i=blockIdx.x*256+threadIdx.x; int s=i/D,d=i%D;
  float acc=0.f;
  for(int j=0;j<=s;++j) acc+=b2f(kv[j*D+d])*b2f(h[d*S+s-j]);
  float y=rb(acc); y=rb(y+rb(b2f(kv[i])*b2f(Dd[d])));
  a[i]=f2b(b2f(q[i])*y);
}
__global__ void k_g1(const u16* x,const u16* wg,const u16* wu,u16* hh){
  int i=blockIdx.x*256+threadIdx.x; int s=i/F,f=i%F;
  float g=0,u=0;
  for(int d=0;d<D;++d){ float xv=b2f(x[s*D+d]); g+=xv*b2f(wg[d*F+f]); u+=xv*b2f(wu[d*F+f]); }
  hh[i]=f2b(rb(g)*rb(u));
}
__global__ void k_gate(const u16* q,const u16* gw,float* g){
  int s=blockIdx.x; float acc=0; for(int d=threadIdx.x;d<D;d+=64) acc+=rb(b2f(q[s*D+d])*b2f(gw[d]));
  for(int o=32;o;o>>=1) acc+=__shfl_xor(acc,o);
  if(threadIdx.x==0){ float z=rb(acc); g[s]=rb(1.f/(1.f+expf(-z))); }
}
__global__ void k_g2(const u16* hh,const u16* wd,const u16* a,const float* g,u16* out){
  int i=blockIdx.x*256+threadIdx.x; int s=i/D,d=i%D;
  float acc=0; for(int f=0;f<F;++f) acc+=b2f(hh[s*F+f])*b2f(wd[f*D+d]);
  float b=rb(acc); float gg=g[s];
  out[i]=f2b(rb(gg*b2f(a[i]))+rb(rb(1.f-gg)*b));
}
extern "C" void kernel_launch(void* const* d_in, const int* in_sizes, int n_in, void* d_out, int out_size, void* d_ws, size_t ws_size, hipStream_t stream){
  const u16* q=(const u16*)d_in[0]; const u16* k=(const u16*)d_in[1]; const u16* v=(const u16*)d_in[2];
  const u16* sk=(const u16*)d_in[3]; const u16* h=(const u16*)d_in[4]; const u16* Dd=(const u16*)d_in[5];
  const u16* x=(const u16*)d_in[0]; const u16* wg=(const u16*)d_in[7]; const u16* wu=(const u16*)d_in[8]; const u16* wd=(const u16*)d_in[9]; const u16* gw=(const u16*)d_in[10];
  char* ws=(char*)d_ws; u16* kv=(u16*)ws; u16* a=(u16*)(ws+(4<<20)); u16* hh=(u16*)(ws+(8<<20)); float* g=(float*)(ws+(16<<20));
  k_kv<<<S*D/256,256,0,stream>>>(k,v,sk,kv);
  k_conv<<<S*D/256,256,0,stream>>>(kv,h,Dd,q,a);
  k_g1<<<S*F/256,256,0,stream>>>(x,wg,wu,hh);
  k_gate<<<S,64,0,stream>>>(q,gw,g);
  k_g2<<<S*D/256,256,0,stream>>>(hh,wd,a,g,(u16*)d_out);
}
```

```cpp
#include <hip/hip_runtime.h>
#include <stdint.h>

namespace gm {
#define LAS __attribute__((address_space(3)))
typedef unsigned short u16;
typedef short bf16x8 __attribute__((ext_vector_type(8)));
typedef short v4i16 __attribute__((ext_vector_type(4)));
typedef float f32x4 __attribute__((ext_vector_type(4)));
typedef float f32x2 __attribute__((ext_vector_type(2)));
typedef __bf16 bf16x2_t __attribute__((ext_vector_type(2)));
typedef unsigned u32x4 __attribute__((ext_vector_type(4)));
typedef unsigned u32x2 __attribute__((ext_vector_type(2)));

constexpr int S = 2048, D = 640, F = 1536;
constexpr int NWG = 256, NTHR = 512;
constexpr int LDS_BYTES = 163840;

constexpr unsigned WS_EPOCH = 0, WS_ARRIVE = 1024, WS_FLAGP = 4096, WS_FLAGH = 8192, WS_FLAGW = 12288, WS_FLAGG = 12544,
                   WS_FLAGY = 16384, WS_TMO = 24576, WS_GATE = 32768, WS_STAMP = 65536;
constexpr unsigned WS_KVR = 1u << 20, WS_YT = 4u << 20, WS_WDP = 8u << 20, WS_H = 12u << 20, WS_END = 20u << 20;

constexpr int G1_SLOT = 45056, G1_WG = 28672, G1_WU = 36864;
constexpr int C_H = 45056, C_HSTR = 9216, C_H2 = 4640;
constexpr int C_K = 90112, C_KSTR = 8448, C_K1 = 4224;
constexpr int G2_HRING = 45056, G2_YT = 110592, G2_RED = 0;
__host__ __device__ constexpr int g2_wslot(int i) { return i < 2 ? i * 20480 : 135168; }

struct Params {
  const u16 *q, *k, *v, *sk, *hk, *dv, *wg, *wu, *wd, *gw;
  u16* out;
  char* ws;
};

#define RLXA __ATOMIC_RELAXED, __HIP_MEMORY_SCOPE_AGENT
__device__ __forceinline__ unsigned ld_dev(const void* p) { return __hip_atomic_load((const unsigned*)p, RLXA); }
__device__ __forceinline__ void st_dev(void* p, unsigned v) { __hip_atomic_store((unsigned*)p, v, RLXA); }
__device__ __forceinline__ unsigned pk(float lo, float hi) { f32x2 v = {lo, hi}; return __builtin_bit_cast(unsigned, __builtin_convertvector(v, bf16x2_t)); }
__device__ __forceinline__ float blo(unsigned x) { return __uint_as_float(x << 16); }
__device__ __forceinline__ float bhi(unsigned x) { return __uint_as_float(x & 0xffff0000u); }
__device__ __forceinline__ float rb(float x) { return blo(pk(x, x)); }
template <int N> __device__ __forceinline__ void wait_vm() { asm volatile("s_waitcnt vmcnt(%0)" ::"n"(N) : "memory"); }
__device__ __forceinline__ void wait_lgkm0() { asm volatile("s_waitcnt lgkmcnt(0)" ::: "memory"); }
__device__ __forceinline__ void bar() { asm volatile("" ::: "memory"); __builtin_amdgcn_s_barrier(); asm volatile("" ::: "memory"); }
template <int AUX> __device__ __forceinline__ void dma16(const void* g, LAS char* l) {
  __builtin_amdgcn_global_load_lds((const unsigned*)g, (LAS unsigned*)l, 16, 0, AUX);
}
__device__ __forceinline__ bf16x8 lds16(const LAS char* p) { return *(const LAS bf16x8*)p; }
__device__ __forceinline__ bf16x8 ldstr(const LAS char* p0, const LAS char* p1) {
  v4i16 a = __builtin_amdgcn_ds_read_tr16_b64_v4i16((LAS v4i16*)p0), b = __builtin_amdgcn_ds_read_tr16_b64_v4i16((LAS v4i16*)p1);
  return __builtin_shufflevector(a, b, 0, 1, 2, 3, 4, 5, 6, 7);
}
template <int OFF> __device__ __forceinline__ void rd128(bf16x8& d, unsigned a) { asm volatile("ds_read_b128 %0, %1 offset:%2" : "=v"(d) : "v"(a), "n"(OFF)); }
template <int OFF> __device__ __forceinline__ void rd64(u32x2& d, unsigned a) { asm volatile("ds_read_b64 %0, %1 offset:%2" : "=v"(d) : "v"(a), "n"(OFF)); }
template <int OFF> __device__ __forceinline__ void rdtr(u32x2& d, unsigned a) { asm volatile("ds_read_b64_tr_b16 %0, %1 offset:%2" : "=v"(d) : "v"(a), "n"(OFF)); }
__device__ __forceinline__ bf16x8 cat(const u32x2& lo, const u32x2& hi) { return __builtin_bit_cast(bf16x8, u32x4{lo[0], lo[1], hi[0], hi[1]}); }
#define SCHED() __builtin_amdgcn_sched_barrier(0)
#define PIN(x) asm volatile("" : "+v"(x))
__device__ __forceinline__ void wait_vm0_seen() { __builtin_amdgcn_s_waitcnt(0x0F70); }
#define MFMA(a, b, c) __builtin_amdgcn_mfma_f32_16x16x32_bf16(a, b, c, 0, 0, 0)

#define STAMP(i) do {} while (0)

__device__ __forceinline__ void poll_flags(const char* flags, int n, unsigned tag, int lane, char* ws) {
#pragma unroll 1
  for (unsigned spins = 0;; ++spins) {
    unsigned f = tag;
    if (lane < n) f = ld_dev(flags + lane * 4);
    if (__all(f == tag)) break;
    if (spins > (1u << 16)) { if (lane == 0) st_dev(ws + WS_TMO, tag); break; }
    __builtin_amdgcn_s_sleep(2);
  }
}

__device__ __forceinline__ unsigned flag_issue(const char* flags, int n, int lane) {
  unsigned f;
  const char* a = flags + (lane < n ? lane : 0) * 4;
  asm volatile("global_load_dword %0, %1, off sc1" : "=v"(f) : "v"(a) : "memory");
  return f;
}
template <int N> __device__ __forceinline__ void wait_vm_for(unsigned& f) { asm volatile("s_waitcnt vmcnt(%1)" : "+v"(f) : "n"(N) : "memory"); }
template <int N> __device__ __forceinline__ void wait_vm_for(unsigned& f, unsigned& g) { asm volatile("s_waitcnt vmcnt(%2)" : "+v"(f), "+v"(g) : "n"(N) : "memory"); }

__device__ __forceinline__ f32x2 up2(unsigned x) { return f32x2{blo(x), bhi(x)}; }
__device__ __forceinline__ void p_tile(const Params& p, int tile, int lane, unsigned tag) {
  const int cbk = tile >> 5, pbk = tile & 31;
  const int pb = lane & 7, cc = lane >> 3;
  const int d0 = cbk * 64 + cc * 8, sb = pbk * 64 + pb * 8;
  u32x4 kr[11], vr[8], wr[4];
#pragma unroll
  for (int i = 0; i < 4; ++i) wr[i] = *(const u32x4*)(p.sk + d0 * 4 + i * 8);
#pragma unroll
  for (int i = 0; i < 11; ++i) { int s = sb - 3 + i; s = s < 0 ? 0 : s; kr[i] = *(const u32x4*)(p.k + (size_t)s * D + d0); }
#pragma unroll
  for (int i = 0; i < 8; ++i) vr[i] = *(const u32x4*)(p.v + (size_t)(sb + i) * D + d0);
  if (sb == 0) { kr[0] = u32x4{0, 0, 0, 0}; kr[1] = kr[0]; kr[2] = kr[0]; }
  auto rs = __builtin_amdgcn_make_buffer_rsrc(p.ws, 0, WS_END, 0x00020000);
#pragma unroll
  for (int c = 0; c < 4; ++c) {
    const f32x2 w0 = {blo(wr[c][0]), blo(wr[c][2])}, w1 = {bhi(wr[c][0]), bhi(wr[c][2])};
    const f32x2 w2 = {blo(wr[c][1]), blo(wr[c][3])}, w3 = {bhi(wr[c][1]), bhi(wr[c][3])};
    f32x2 kf[11], o[8];
#pragma unroll
    for (int i = 0; i < 11; ++i) kf[i] = up2(kr[i][c]);
#pragma unroll
    for (int i = 0; i < 8; ++i) o[i] = (w3 * kf[i] + w2 * kf[i + 1] + w1 * kf[i + 2] + w0 * kf[i + 3]) * up2(vr[i][c]);
    const u32x4 lo = {pk(o[7].x, o[6].x), pk(o[5].x, o[4].x), pk(o[3].x, o[2].x), pk(o[1].x, o[0].x)};
    const u32x4 hi = {pk(o[7].y, o[6].y), pk(o[5].y, o[4].y), pk(o[3].y, o[2].y), pk(o[1].y, o[0].y)};
    __builtin_amdgcn_raw_buffer_store_b128(lo, rs, WS_KVR + ((d0 + 2 * c) * S + (S - 8 - sb)) * 2, 0, 16);
    __builtin_amdgcn_raw_buffer_store_b128(hi, rs, WS_KVR + ((d0 + 2 * c + 1) * S + (S - 8 - sb)) * 2, 0, 16);
  }
  wait_vm<0>();
  if (lane == 0) st_dev(p.ws + WS_FLAGP + cbk * 128 + pbk * 4, tag);
}

__device__ __forceinline__ void aux_repack(const Params& p, LAS char* lds, int a, int wid, int lane, unsigned tag) {
#pragma unroll 1
  for (int i = wid; i < 120; i += 8) {
    const int kk = i / 40, c_lin = (i % 40) * 64 + lane, row = c_lin / 80, cp = c_lin % 80;
    const int c = (cp & ~15) | ((cp & 15) ^ ((row & 7) << 1));
    dma16<0>(p.wd + (size_t)((3 * a + kk) * 32 + row) * D + c * 8, lds + i * 1024);
  }
  wait_vm0_seen();
  bar();
  auto rs = __builtin_amdgcn_make_buffer_rsrc(p.ws, 0, WS_END, 0x00020000);
  const int g = lane >> 4, qq = (lane & 15) >> 2, pp = lane & 3;
#pragma unroll 5
  for (int ii = 0; ii < 15; ++ii) {
    const int i = wid + 8 * ii;
    const int kk = i / 40, blk = i % 40, cb8 = blk / 5, ct = blk % 5;
    const int c = 10 * cb8 + 2 * ct + (pp >> 1);
    const int r0 = 4 * g + qq, r1 = r0 + 16;
    const int a0 = kk * 40960 + r0 * 1280 + (((c & ~15) | ((c & 15) ^ ((r0 & 7) << 1))) << 4) + 8 * (pp & 1);
    const int a1 = kk * 40960 + r1 * 1280 + (((c & ~15) | ((c & 15) ^ ((r1 & 7) << 1))) << 4) + 8 * (pp & 1);
    const bf16x8 f = ldstr(lds + a0, lds + a1);
    __builtin_amdgcn_raw_buffer_store_b128(__builtin_bit_cast(u32x4, f), rs, WS_WDP + (((cb8 * 48 + 3 * a + kk) * 5 + ct) * 64 + lane) * 16, 0, 16);
  }
  wait_vm<0>();
  bar();
  if (wid == 0 && lane == 0) st_dev(p.ws + WS_FLAGW + a * 4, tag);
}

__device__ __forceinline__ void aux_gate(const Params& p, int row0, int a, int wid, int lane, unsigned tag) {
  const int r4 = lane >> 4, c16 = lane & 15;
  float gw[5][8];
#pragma unroll
  for (int i = 0; i < 5; ++i) {
    const u32x4 t = *(const u32x4*)(p.gw + (c16 + 16 * i) * 8);
#pragma unroll
    for (int j = 0; j < 4; ++j) { gw[i][2 * j] = blo(t[j]); gw[i][2 * j + 1] = bhi(t[j]); }
  }
  u32x4 x[4][5];
#pragma unroll
  for (int it = 0; it < 4; ++it)
#pragma unroll
    for (int i = 0; i < 5; ++i) x[it][i] = *(const u32x4*)(p.q + (size_t)(row0 + wid * 16 + it * 4 + r4) * D + (c16 + 16 * i) * 8);
#pragma unroll
  for (int it = 0; it < 4; ++it) {
    float z = 0.f;
#pragma unroll
    for (int i = 0; i < 5; ++i)
#pragma unroll
      for (int j = 0; j < 4; ++j) z += blo(x[it][i][j]) * gw[i][2 * j] + bhi(x[it][i][j]) * gw[i][2 * j + 1];
    z += __shfl_xor(z, 1); z += __shfl_xor(z, 2); z += __shfl_xor(z, 4); z += __shfl_xor(z, 8);
    z = rb(z);
    const float gt = rb(1.f / (1.f + __expf(-z)));
    if (c16 == 0) st_dev(p.ws + WS_GATE + (row0 + wid * 16 + it * 4 + r4) * 4, __float_as_uint(gt));
  }
  wait_vm<0>();
  bar();
  if (wid == 0 && lane == 0) st_dev(p.ws + WS_FLAGG + a * 4, tag);
}

template <int NX>
__device__ __forceinline__ void g1_issue(const Params& p, LAS char* slot, int ks, int row0, int cb, int lw, unsigned xoff, unsigned woff0, unsigned woff1) {
  const char* xb = (const char*)p.q + (size_t)row0 * (D * 2) + ks * 128;
  const size_t wo = (size_t)(64 * ks + 16 * lw) * (F * 2) + cb * 128;
  dma16<0>((const char*)p.wg + wo + woff0, slot + G1_WG + (2 * lw) * 1024);
  dma16<0>((const char*)p.wu + wo + woff0, slot + G1_WU + (2 * lw) * 1024);
  dma16<0>((const char*)p.wg + wo + 8 * (F * 2) + woff1, slot + G1_WG + (2 * lw + 1) * 1024);
  dma16<0>((const char*)p.wu + wo + 8 * (F * 2) + woff1, slot + G1_WU + (2 * lw + 1) * 1024);
#pragma unroll
  for (int i = 0; i < NX; ++i) dma16<0>(xb + (size_t)(lw + 4 * i) * (8 * D * 2) + xoff, slot + (lw + 4 * i) * 1024);
}
__device__ __forceinline__ unsigned g1_xoff(int lane) { return (lane >> 3) * (D * 2) + (((lane & 7) ^ (lane >> 3)) << 4); }
__device__ __forceinline__ unsigned g1_woff(int lane, int par) { return (lane >> 3) * (F * 2) + (((lane & 7) ^ ((((lane >> 4) & 1) | (par << 1)) << 1)) << 4); }

template <int NX>
__device__ __forceinline__ void g1_loader(const Params& p, LAS char* lds, int row0, int cb, int rot, bool conv, int ch0, int lw, int lane, unsigned tag) {
  constexpr int NP = NX + 4;
  const unsigned xoff = g1_xoff(lane), woff0 = g1_woff(lane, 0), woff1 = g1_woff(lane, 1);
  int ks = rot + 2; ks = ks >= 10 ? ks - 10 : ks;
  int slot = 2 * G1_SLOT;
#pragma unroll 1
  for (int s = 0; s < 8; ++s) {
    wait_vm<NP>();
    bar();
    g1_issue<NX>(p, lds + slot, ks, row0, cb, lw, xoff, woff0, woff1);
    ks = ks == 9 ? 0 : ks + 1;
    slot = slot == 2 * G1_SLOT ? 0 : slot + G1_SLOT;
  }
  wait_vm<NP>();
  bar();
  unsigned fp = tag;
  if (conv) {
    fp = flag_issue(p.ws + WS_FLAGP + (ch0 >> 6) * 128, 32, lane);
    const u16* src = p.hk + (size_t)(ch0 + lw) * S;
#pragma unroll
    for (int a = 0; a < 2; ++a)
#pragma unroll
      for (int j = 0; j < 4; ++j) {
        int e = 8 * (64 * j + lane) + 2 * a - 2; e = e < 0 ? 0 : e;
        dma16<0>(src + e, lds + C_H + lw * C_HSTR + a * C_H2 + 448 + j * 1024);
      }
    wait_vm_for<8>(fp);
    if (!__all(fp == tag)) poll_flags(p.ws + WS_FLAGP + (ch0 >> 6) * 128, 32, tag, lane, p.ws);
  } else wait_vm<0>();
  bar();
  if (conv) {
    const u16* src = (const u16*)(p.ws + WS_KVR) + (size_t)(ch0 + lw) * S;
#pragma unroll
    for (int j = 0; j < 4; ++j) dma16<16>(src + 8 * (64 * j + lane), lds + C_K + lw * C_KSTR + C_K1 + 16 + j * 1024);
#pragma unroll
    for (int j = 0; j < 4; ++j) dma16<16>(src + 1 + 8 * (64 * j + lane), lds + C_K + lw * C_KSTR + 16 + j * 1024);
  }
}

template <int NRT> struct G1Frag { u32x2 wl[4], wh[4]; bf16x8 x[NRT]; };
template <int NRT, int KK, int I>
__device__ __forceinline__ void g1_read1(G1Frag<NRT>& f, unsigned wa0, unsigned wa1, unsigned xa) {
  if constexpr (I == 0) rdtr<KK * 4096>(f.wl[0], wa0);
  else if constexpr (I == 1) rdtr<KK * 4096 + 512>(f.wh[0], wa0);
  else if constexpr (I == 2) rdtr<KK * 4096 + 8192>(f.wl[2], wa0);
  else if constexpr (I == 3) rdtr<KK * 4096 + 8192 + 512>(f.wh[2], wa0);
  else if constexpr (I == 4) rdtr<KK * 4096>(f.wl[1], wa1);
  else if constexpr (I == 5) rdtr<KK * 4096 + 512>(f.wh[1], wa1);
  else if constexpr (I == 6) rdtr<KK * 4096 + 8192>(f.wl[3], wa1);
  else if constexpr (I == 7) rdtr<KK * 4096 + 8192 + 512>(f.wh[3], wa1);
  else rd128<(I - 8) * 2048>(f.x[I - 8], xa);
}
template <int NRT> __device__ __forceinline__ void g1_wait(G1Frag<NRT>& f) {
  asm volatile("s_waitcnt lgkmcnt(0)" : "+v"(f.wl[0]), "+v"(f.wh[0]), "+v"(f.wl[1]), "+v"(f.wh[1]), "+v"(f.wl[2]), "+v"(f.wh[2]), "+v"(f.wl[3]), "+v"(f.wh[3]),
               "+v"(f.x[0]), "+v"(f.x[1]), "+v"(f.x[2]), "+v"(f.x[3])::"memory");
  if constexpr (NRT > 4) asm volatile("" : "+v"(f.x[4]), "+v"(f.x[5]), "+v"(f.x[6]));
}
template <int NRT, int KK, int N>
__device__ __forceinline__ void g1_step(const G1Frag<NRT>& c, G1Frag<NRT>& nx, f32x4 (&ag)[NRT][2], f32x4 (&au)[NRT][2], unsigned wa0, unsigned wa1, unsigned xa) {
  if constexpr (N < 8 + NRT) g1_read1<NRT, KK, N>(nx, wa0, wa1, xa);
  constexpr int m = N / 4, j = N % 4;
  if constexpr (j == 0) ag[m][0] = MFMA(cat(c.wl[0], c.wh[0]), c.x[m], ag[m][0]);
  else if constexpr (j == 1) au[m][0] = MFMA(cat(c.wl[2], c.wh[2]), c.x[m], au[m][0]);
  else if constexpr (j == 2) ag[m][1] = MFMA(cat(c.wl[1], c.wh[1]), c.x[m], ag[m][1]);
  else au[m][1] = MFMA(cat(c.wl[3], c.wh[3]), c.x[m], au[m][1]);
  SCHED();
  if constexpr (N + 1 < 4 * NRT) g1_step<NRT, KK, N + 1>(c, nx, ag, au, wa0, wa1, xa);
}

template <int NRT>
__device__ __forceinline__ void g1_consumer(const Params& p, LAS char* lds, int row0, int cb, int wid, int lane) {
  const int cg = wid & 1, rt0 = (wid >> 1) * NRT, g = lane >> 4, row = lane & 15, qq = row >> 2, pp = lane & 3;
  const int sw = (qq >> 1) | ((g & 1) << 1);
  const unsigned lb = (unsigned)(size_t)lds;
  const unsigned xa0 = lb + rt0 * 2048 + row * 128 + ((g ^ (row & 7)) << 4), xa1 = xa0 ^ 64;
  const unsigned wa0 = lb + G1_WG + (8 * g + qq) * 128 + 32 * ((2 * cg) ^ sw) + 8 * pp;
  const unsigned wa1 = lb + G1_WG + (8 * g + qq) * 128 + 32 * ((2 * cg + 1) ^ sw) + 8 * pp;
  f32x4 ag[NRT][2], au[NRT][2];
  G1Frag<NRT> A, B;
#pragma unroll
  for (int m = 0; m < NRT; ++m) { ag[m][0] = f32x4{0, 0, 0, 0}; ag[m][1] = ag[m][0]; au[m][0] = ag[m][0]; au[m][1] = ag[m][0]; B.x[m] = bf16x8{0, 0, 0, 0, 0, 0, 0, 0}; }
#pragma unroll
  for (int n = 0; n < 4; ++n) { B.wl[n] = u32x2{0, 0}; B.wh[n] = u32x2{0, 0}; }
  int slot = 0;
#pragma unroll 1
  for (int s = 0; s < 10; ++s) {
    bar();
    g1_step<NRT, 0, 0>(B, A, ag, au, wa0 + slot, wa1 + slot, xa0 + slot);
    g1_wait(A);
    g1_step<NRT, 1, 0>(A, B, ag, au, wa0 + slot, wa1 + slot, xa1 + slot);
    g1_wait(B);
    slot = slot == 2 * G1_SLOT ? 0 : slot + G1_SLOT;
  }
#pragma unroll
  for (int m = 0; m < NRT; ++m) {
    ag[m][0] = MFMA(cat(B.wl[0], B.wh[0]), B.x[m], ag[m][0]);
    au[m][0] = MFMA(cat(B.wl[2], B.wh[2]), B.x[m], au[m][0]);
    ag[m][1] = MFMA(cat(B.wl[1], B.wh[1]), B.x[m], ag[m][1]);
    au[m][1] = MFMA(cat(B.wl[3], B.wh[3]), B.x[m], au[m][1]);
  }
  auto rs = __builtin_amdgcn_make_buffer_rsrc(p.ws, 0, WS_END, 0x00020000);
#pragma unroll
  for (int m = 0; m < NRT; ++m) {
    u32x4 w;
#pragma unroll
    for (int n = 0; n < 2; ++n) {
      const unsigned g01 = pk(ag[m][n][0], ag[m][n][1]), g23 = pk(ag[m][n][2], ag[m][n][3]);
      const unsigned u01 = pk(au[m][n][0], au[m][n][1]), u23 = pk(au[m][n][2], au[m][n][3]);
      w[2 * n] = pk(blo(g01) * blo(u01), bhi(g01) * bhi(u01));
      w[2 * n + 1] = pk(blo(g23) * blo(u23), bhi(g23) * bhi(u23));
    }
    const int rtile = (row0 >> 4) + rt0 + m;
    __builtin_amdgcn_raw_buffer_store_b128(w, rs, WS_H + ((rtile * 48 + 2 * cb + cg) * 64 + lane) * 16, 0, 16);
  }
}

struct CSet { bf16x8 a[8]; u32x2 bl[8], bh[8]; };
template <int R, int K> __device__ __forceinline__ void conv_read1(CSet& s, unsigned A0, unsigned B0) {
  if constexpr (K < 8) rd128<64 * R + 512 * K>(s.a[K], A0);
  else if constexpr (((K - 8) & 1) == 0) rd64<64 * R + 512 * (7 - (K - 8) / 2)>(s.bl[(K - 8) / 2], B0);
  else rd64<64 * R + 512 * (7 - (K - 8) / 2) + 8>(s.bh[(K - 8) / 2], B0);
}
template <int R, int K> __device__ __forceinline__ void conv_reads(CSet& s, unsigned A0, unsigned B0) {
  conv_read1<R, K>(s, A0, B0);
  if constexpr (K < 23) conv_reads<R, K + 1>(s, A0, B0);
}
__device__ __forceinline__ void conv_wait(CSet& s) {
  asm volatile("s_waitcnt lgkmcnt(0)" : "+v"(s.a[0]), "+v"(s.a[1]), "+v"(s.a[2]), "+v"(s.a[3]), "+v"(s.a[4]), "+v"(s.a[5]), "+v"(s.a[6]), "+v"(s.a[7]),
               "+v"(s.bl[0]), "+v"(s.bl[1]), "+v"(s.bl[2]), "+v"(s.bl[3]), "+v"(s.bl[4]), "+v"(s.bl[5]), "+v"(s.bl[6]), "+v"(s.bl[7])::"memory");
  asm volatile("" : "+v"(s.bh[0]), "+v"(s.bh[1]), "+v"(s.bh[2]), "+v"(s.bh[3]), "+v"(s.bh[4]), "+v"(s.bh[5]), "+v"(s.bh[6]), "+v"(s.bh[7]));
}
constexpr int conv_pa(int n) { int a = 0; while (n >= 8 - a) { n -= 8 - a; ++a; } return a; }
constexpr int conv_pe(int n) { int a = 0; while (n >= 8 - a) { n -= 8 - a; ++a; } return n; }
template <int R, int N> __device__ __forceinline__ void conv_step(const CSet& c, CSet& nx, f32x4 (&acc)[8], unsigned A0, unsigned B0) {
  if constexpr (R < 7 && N < 24) conv_read1<R + 1, N>(nx, A0, B0);
  constexpr int a = conv_pa(N), e = conv_pe(N);
  acc[a + e] = MFMA(c.a[a], cat(c.bl[e], c.bh[e]), acc[a + e]);
  SCHED();
  if constexpr (N < 35) conv_step<R, N + 1>(c, nx, acc, A0, B0);
}
template <int R> __device__ __forceinline__ void conv_res(const CSet& c, CSet& nx, f32x4 (&acc)[8], unsigned A0, unsigned B0) {
  conv_step<R, 0>(c, nx, acc, A0, B0);
  if constexpr (R < 7) conv_wait(nx);
}

__device__ __forceinline__ void conv_channel(const Params& p, LAS char* lds, int ch, int c, int lane, unsigned tag) {
  LAS char* H = lds + C_H + c * C_HSTR;
  LAS char* K = lds + C_K + c * C_KSTR;
  {
    const u32x4 z = {0, 0, 0, 0};
    if (lane < 56) *(LAS u32x4*)(H + (lane >= 28 ? C_H2 + (lane - 28) * 16 : lane * 16)) = z;
    if (lane < 8) *(LAS u32x4*)(K + (lane >> 2) * C_K1 + (257 + (lane & 3)) * 16) = z;
    if (lane == 0) {
      const u32x4 t = *(LAS u32x4*)(H + C_H2 + 448);
      *(LAS u32x4*)(H + 448) = u32x4{0u, t[0], t[1], t[2]};
      *(LAS u16*)(K + 14) = *(LAS u16*)(K + C_K1 + 16);
      *(LAS u16*)(K + 2055 * 2) = 0;
    }
    wait_lgkm0();
    __builtin_amdgcn_sched_barrier(0);
  }
  const int rc = lane & 15, g = lane >> 4;
  const unsigned A0 = (unsigned)(size_t)H + (rc & 1) * C_H2 + 64 * (rc >> 1) + 16 * g;
  const unsigned B0 = (unsigned)(size_t)K + (rc & 1) * C_K1 + 72 - 8 * (rc >> 1) + 16 * g;
  f32x4 acc[8];
#pragma unroll
  for (int t = 0; t < 8; ++t) acc[t] = f32x4{0, 0, 0, 0};
  CSet X, Y2;
  conv_reads<0, 0>(X, A0, B0);
  conv_wait(X);
  conv_res<0>(X, Y2, acc, A0, B0);
  conv_res<1>(Y2, X, acc, A0, B0);
  conv_res<2>(X, Y2, acc, A0, B0);
  conv_res<3>(Y2, X, acc, A0, B0);
  conv_res<4>(X, Y2, acc, A0, B0);
  conv_res<5>(Y2, X, acc, A0, B0);
  conv_res<6>(X, Y2, acc, A0, B0);
  conv_res<7>(Y2, X, acc, A0, B0);
  asm volatile("" ::: "memory");
  LAS char* Y = H;
  const bool odd = rc & 1;
#pragma unroll
  for (int t = 0; t < 8; ++t) {
    const float s0 = odd ? acc[t][0] : acc[t][2], s1 = odd ? acc[t][1] : acc[t][3];
    const float t0 = __shfl_xor(s0, 1), t1 = __shfl_xor(s1, 1);
    const u32x2 w = odd ? u32x2{pk(t0, acc[t][2]), pk(t1, acc[t][3])} : u32x2{pk(acc[t][0], t0), pk(acc[t][1], t1)};
    *(LAS u32x2*)(Y + 2 * (256 * t + 64 * g + 32 * (rc & 1) + 4 * (rc >> 1))) = w;
  }
  wait_lgkm0();
  __builtin_amdgcn_sched_barrier(0);
  const float dd = blo(p.dv[ch]);
  auto rs = __builtin_amdgcn_make_buffer_rsrc(p.ws, 0, WS_END, 0x00020000);
#pragma unroll
  for (int it = 0; it < 4; ++it) {
    const int pos = 8 * (lane + 64 * it);
    const u32x4 y = *(const LAS u32x4*)(Y + 2 * pos);
    const u32x4 kv = *(const LAS u32x4*)(K + C_K1 + 2 * (2048 - pos));
    u32x4 o;
#pragma unroll
    for (int j = 0; j < 4; ++j) {
      const unsigned kk = kv[3 - j];
      o[j] = pk(blo(y[j]) + rb(bhi(kk) * dd), bhi(y[j]) + rb(blo(kk) * dd));
    }
    __builtin_amdgcn_raw_buffer_store_b128(o, rs, WS_YT + (ch * S + pos) * 2, 0, 16);
  }
  wait_vm<0>();
  if (lane == 0) st_dev(p.ws + WS_FLAGY + (ch / 80) * 512 + (ch % 80) * 4, tag);
}

__device__ __forceinline__ void g2_issue_w(const char* wsrc, LAS char* lds, int i, int lw) {
  LAS char* d = lds + g2_wslot(i % 3);
#pragma unroll
  for (int j = 0; j < 5; ++j) dma16<16>(wsrc + i * 20480 + (lw * 5 + j) * 1024, d + (lw * 5 + j) * 1024);
}
__device__ __forceinline__ void g2_issue_h(const char* hsrc, LAS char* lds, int i, int lw) {
#pragma unroll
  for (int j = 0; j < 4; ++j) dma16<16>(hsrc + i * 4096 + j * 1024, lds + G2_HRING + (i & 3) * 16384 + lw * 4096 + j * 1024);
}
__device__ __forceinline__ void g2_loader(const Params& p, LAS char* lds, int row0, int cb8, int lw, int lane, unsigned tag) {
  const char* hsrc = p.ws + WS_H + (size_t)((row0 >> 4) + lw) * 48 * 1024 + lane * 16;
  const char* wsrc = p.ws + WS_WDP + (size_t)cb8 * 48 * 5120 + lane * 16;
#pragma unroll
  for (int i = 0; i < 4; ++i) g2_issue_h(hsrc, lds, i, lw);
  wait_vm<12>(); bar();
  wait_vm<8>(); bar();
  g2_issue_w(wsrc, lds, 3, lw); g2_issue_h(hsrc, lds, 4, lw);
  unsigned fy0 = tag, fy1 = tag;
#pragma unroll 1
  for (int i = 2; i <= 8; ++i) {
    wait_vm<13>(); bar();
    g2_issue_w(wsrc, lds, i + 2, lw); g2_issue_h(hsrc, lds, i + 3, lw);
    if (i == 6) { fy0 = flag_issue(p.ws + WS_FLAGY + cb8 * 512, 64, lane); fy1 = flag_issue(p.ws + WS_FLAGY + cb8 * 512 + 256, 16, lane); }
  }
  wait_vm_for<13>(fy0, fy1);
  if (!__all(fy0 == tag && fy1 == tag)) { poll_flags(p.ws + WS_FLAGY + cb8 * 512, 64, tag, lane, p.ws); poll_flags(p.ws + WS_FLAGY + cb8 * 512 + 256, 16, tag, lane, p.ws); }
  bar();
  g2_issue_w(wsrc, lds, 11, lw);
  {
    const int rl = lane >> 3, c = (lane & 7) ^ (((rl >> 1) & 3) << 1);
    const char* y = p.ws + WS_YT + ((size_t)(cb8 * 80 + rl) * S + row0 + c * 8) * 2;
#pragma unroll
    for (int j = 0; j < 3; ++j) { const int pc = min(lw * 3 + j, 9); dma16<16>(y + (size_t)pc * 8 * S * 2, lds + G2_YT + pc * 1024); }
  }
  wait_vm<12>(); bar();
  wait_vm<3>(); bar();
  wait_vm<0>(); bar();
}

struct G2Frag { bf16x8 w[2][5], h[2]; };
template <int I, int HALF, int N> __device__ __forceinline__ void g2_read1(G2Frag& f, unsigned wa, unsigned ha) {
  constexpr int H = (I & 3) * 16384 + HALF * 2048, k = N / 6, j = N % 6;
  if constexpr (j == 0) rd128<H + k * 1024>(f.h[k], ha);
  else rd128<(2 * HALF + k) * 5120 + (j - 1) * 1024>(f.w[k][j - 1], wa);
}
template <int I, int HALF, int N> __device__ __forceinline__ void g2_step(const G2Frag& c, G2Frag& nx, f32x4 (&acc)[5], unsigned wa, unsigned ha, bool mul) {
  g2_read1<I, HALF, N>(nx, wa, ha);
  if constexpr (N < 10) { if (mul) acc[N % 5] = MFMA(c.w[N / 5][N % 5], c.h[N / 5], acc[N % 5]); }
  SCHED();
  if constexpr (N < 11) g2_step<I, HALF, N + 1>(c, nx, acc, wa, ha, mul);
}
__device__ __forceinline__ void g2_wait(G2Frag& f) {
  asm volatile("s_waitcnt lgkmcnt(0)" : "+v"(f.h[0]), "+v"(f.h[1]), "+v"(f.w[0][0]), "+v"(f.w[0][1]), "+v"(f.w[0][2]), "+v"(f.w[0][3]), "+v"(f.w[0][4]),
               "+v"(f.w[1][0]), "+v"(f.w[1][1]), "+v"(f.w[1][2]), "+v"(f.w[1][3]), "+v"(f.w[1][4])::"memory");
}
template <int I> __device__ __forceinline__ void g2_iter(G2Frag& X, G2Frag& Y, f32x4 (&acc)[5], unsigned wa, unsigned ha) {
  bar();
  const unsigned w = wa + g2_wslot(I % 3);
  g2_step<I, 0, 0>(Y, X, acc, w, ha, I > 0);
  g2_wait(X);
  g2_step<I, 1, 0>(X, Y, acc, w, ha, true);
  g2_wait(Y);
  if constexpr (I < 11) g2_iter<I + 1>(X, Y, acc, wa, ha);
}

__device__ __forceinline__ void g2_consumer(const Params& p, LAS char* lds, int row0, int cb8, int rt, int lane) {
  const int g = lane >> 4, j = lane & 15;
  const int prow = row0 + rt * 16 + j;
  u32x2 qv[5];
#pragma unroll
  for (int ct = 0; ct < 5; ++ct) qv[ct] = *(const u32x2*)(p.q + (size_t)prow * D + cb8 * 80 + ct * 16 + 4 * g);
  const float gt = __uint_as_float(ld_dev(p.ws + WS_GATE + prow * 4));
  const unsigned lb = (unsigned)(size_t)lds;
  f32x4 acc[5];
#pragma unroll
  for (int ct = 0; ct < 5; ++ct) acc[ct] = f32x4{0, 0, 0, 0};
  G2Frag X, Y;
  g2_iter<0>(X, Y, acc, lb + lane * 16, lb + G2_HRING + rt * 4096 + lane * 16);
#pragma unroll
  for (int n = 0; n < 10; ++n) acc[n % 5] = MFMA(Y.w[n / 5][n % 5], Y.h[n / 5], acc[n % 5]);
  bar();
  const int qq = j >> 2, pp = lane & 3;
  const float omg = rb(1.f - gt);
#pragma unroll
  for (int ct = 0; ct < 5; ++ct) {
    const int yr = 16 * ct + 4 * g + qq, yc = 2 * rt + (pp >> 1);
    const v4i16 yv = __builtin_amdgcn_ds_read_tr16_b64_v4i16((LAS v4i16*)(lds + G2_YT + yr * 128 + ((yc ^ (((yr >> 1) & 3) << 1)) << 4) + 8 * (pp & 1)));
    const u32x2 y2 = __builtin_bit_cast(u32x2, yv);
    const f32x4 b = acc[ct];
    const float a0 = rb(blo(qv[ct][0]) * blo(y2[0])), a1 = rb(bhi(qv[ct][0]) * bhi(y2[0])), a2 = rb(blo(qv[ct][1]) * blo(y2[1])), a3 = rb(bhi(qv[ct][1]) * bhi(y2[1]));
    u32x2 w;
    w[0] = pk(rb(gt * a0) + rb(omg * rb(b[0])), rb(gt * a1) + rb(omg * rb(b[1])));
    w[1] = pk(rb(gt * a2) + rb(omg * rb(b[2])), rb(gt * a3) + rb(omg * rb(b[3])));
    *(u32x2*)(p.out + (size_t)prow * D + cb8 * 80 + ct * 16 + 4 * g) = w;
  }
}

__global__ __launch_bounds__(NTHR, 2) void gm_kernel(Params p) {
  extern __shared__ __attribute__((aligned(16))) char shm[];
  LAS char* lds = (LAS char*)shm;
  const int tid = threadIdx.x, wid = __builtin_amdgcn_readfirstlane(tid >> 6), lane = tid & 63;
  const int b = blockIdx.x, xcd = b & 7, r = b >> 3;
  STAMP(0);
  unsigned epoch;
  asm volatile("s_load_dword %0, %1, 0x0" : "=s"(epoch) : "s"(p.ws) : "memory");

  const bool is_g1 = r < 30;
  const int rg = r % 5, cs = r / 5;
  const bool big = is_g1 && rg < 4;
  const int bigidx = cs * 4 + rg;
  const bool conv = big && bigidx < 20;
  const int ch0 = xcd * 80 + bigidx * 4;

  const int row0 = (xcd >> 2) * 1024 + rg * 224, cb = 6 * (xcd & 3) + cs;
  const int rot = (r * 3 + xcd) % 10;
  if (is_g1 && wid >= 4) {
    const unsigned xoff = g1_xoff(lane), woff0 = g1_woff(lane, 0), woff1 = g1_woff(lane, 1);
    const int k1 = rot == 9 ? 0 : rot + 1;
    if (big) { g1_issue<7>(p, lds, rot, row0, cb, wid - 4, xoff, woff0, woff1); g1_issue<7>(p, lds + G1_SLOT, k1, row0, cb, wid - 4, xoff, woff0, woff1); }
    else { g1_issue<4>(p, lds, rot, row0, cb, wid - 4, xoff, woff0, woff1); g1_issue<4>(p, lds + G1_SLOT, k1, row0, cb, wid - 4, xoff, woff0, woff1); }
  }
  asm volatile("s_waitcnt lgkmcnt(0)" : "+s"(epoch)::"memory");
  const unsigned tag = epoch + 1u;
  if (tid == 0) st_dev(p.ws + WS_ARRIVE + b * 4, tag);
  if (!big) {
    const int pi = xcd * 8 + (is_g1 ? cs : r - 24);
    if (wid < 5) p_tile(p, pi * 5 + wid, lane, tag);
  }
  STAMP(1);
  if (is_g1) {
    if (wid >= 4) {
      if (big) g1_loader<7>(p, lds, row0, cb, rot, conv, ch0, wid - 4, lane, tag);
      else g1_loader<4>(p, lds, row0, cb, rot, false, 0, wid - 4, lane, tag);
    } else {
      if (big) g1_consumer<7>(p, lds, row0, cb, wid, lane);
      else g1_consumer<4>(p, lds, row0, cb, wid, lane);
    }
    STAMP(2);
    wait_vm<0>();
    bar();
    if (tid == 0) st_dev(p.ws + WS_FLAGH + ((xcd >> 2) * 5 + rg) * 128 + cb * 4, tag);
  } else {
    const int a = 2 * xcd + (r - 30);
    aux_repack(p, lds, a, wid, lane, tag);
    aux_gate(p, xcd * 256 + (r - 30) * 128, a, wid, lane, tag);
    STAMP(2);
    if (b == 240 && wid == 0) {
      for (int i = 0; i < 4; ++i) poll_flags(p.ws + WS_ARRIVE + i * 256, 64, tag, lane, p.ws);
      if (lane == 0) st_dev(p.ws + WS_EPOCH, tag);
    }
    bar();
  }
  STAMP(3);
  const int cb8 = r >> 2, orow0 = xcd * 256 + (r & 3) * 64;
  if (wid < 4) {
    if (conv) conv_channel(p, lds, ch0 + wid, wid, lane, tag);
  } else {
    if (wid == 4) poll_flags(p.ws + WS_FLAGW, 16, tag, lane, p.ws);
    if (wid != 4) poll_flags(p.ws + WS_FLAGW, 16, tag, lane, p.ws);
    const char* wsrc = p.ws + WS_WDP + (size_t)cb8 * 48 * 5120 + lane * 16;
#pragma unroll 1
    for (int i = wid - 4; i < 60; i += 4) { const int st = i / 20, pc = i % 20; dma16<16>(wsrc + st * 20480 + pc * 1024, lds + g2_wslot(st) + pc * 1024); }
    if (wid == 4) {
      poll_flags(p.ws + WS_FLAGG, 16, tag, lane, p.ws);
      const int half = orow0 >> 10, loc = orow0 & 1023;
      const int g0 = min(loc / 224, 4), g1 = min((loc + 63) / 224, 4);
      poll_flags(p.ws + WS_FLAGH + (half * 5 + g0) * 128, 24, tag, lane, p.ws);
      if (g1 != g0) poll_flags(p.ws + WS_FLAGH + (half * 5 + g1) * 128, 24, tag, lane, p.ws);
    }
    wait_vm<0>();
  }
  STAMP(4);
  bar();
  STAMP(5);
  if (wid < 4) g2_consumer(p, lds, orow0, cb8, wid, lane);
  else g2_loader(p, lds, orow0, cb8, wid - 4, lane, tag);
  STAMP(6);
}
}

extern "C" void kernel_launch(void* const* d_in, const int* in_sizes, int n_in, void* d_out, int out_size, void* d_ws, size_t ws_size, hipStream_t stream) {
  using namespace gm;
  Params p;
  p.q = (const u16*)d_in[0]; p.k = (const u16*)d_in[1]; p.v = (const u16*)d_in[2]; p.sk = (const u16*)d_in[3];
  p.hk = (const u16*)d_in[4]; p.dv = (const u16*)d_in[5]; p.wg = (const u16*)d_in[7]; p.wu = (const u16*)d_in[8];
  p.wd = (const u16*)d_in[9]; p.gw = (const u16*)d_in[10]; p.out = (u16*)d_out; p.ws = (char*)d_ws;
  (void)hipFuncSetAttribute((const void*)gm_kernel, hipFuncAttributeMaxDynamicSharedMemorySize, LDS_BYTES);
  gm_kernel<<<NWG, NTHR, LDS_BYTES, stream>>>(p);
}
```

```cpp
#include <hip/hip_runtime.h>
#include <stdint.h>

namespace gm {
#define LAS __attribute__((address_space(3)))
typedef unsigned short u16;
typedef short bf16x8 __attribute__((ext_vector_type(8)));
typedef short v4i16 __attribute__((ext_vector_type(4)));
typedef float f32x4 __attribute__((ext_vector_type(4)));
typedef float f32x2 __attribute__((ext_vector_type(2)));
typedef __bf16 bf16x2_t __attribute__((ext_vector_type(2)));
typedef unsigned u32x4 __attribute__((ext_vector_type(4)));
typedef unsigned u32x2 __attribute__((ext_vector_type(2)));

constexpr int S = 2048, D = 640, F = 1536;
constexpr int NWG = 256, NTHR = 512;
constexpr int LDS_BYTES = 163840;

constexpr unsigned WS_EPOCH = 0, WS_ARRIVE = 1024, WS_FLAGP = 4096, WS_FLAGH = 8192, WS_FLAGW = 12288, WS_FLAGG = 12544,
                   WS_FLAGY = 16384, WS_TMO = 24576, WS_GATE = 32768, WS_STAMP = 65536;
constexpr unsigned WS_KVR = 1u << 20, WS_YT = 4u << 20, WS_WDP = 8u << 20, WS_H = 12u << 20, WS_END = 20u << 20;

constexpr int G1_SLOT = 45056, G1_WG = 28672, G1_WU = 36864;
constexpr int C_H = 45056, C_HSTR = 9216, C_H2 = 4640;
constexpr int C_K = 90112, C_KSTR = 8448, C_K1 = 4224;
constexpr int G2_HRING = 45056, G2_YT = 110592, G2_RED = 0;
__host__ __device__ constexpr int g2_wslot(int i) { return i < 2 ? i * 20480 : 135168; }

struct Params {
  const u16 *q, *k, *v, *sk, *hk, *dv, *wg, *wu, *wd, *gw;
  u16* out;
  char* ws;
};

#define RLXA __ATOMIC_RELAXED, __HIP_MEMORY_SCOPE_AGENT
__device__ __forceinline__ unsigned ld_dev(const void* p) { return __hip_atomic_load((const unsigned*)p, RLXA); }
__device__ __forceinline__ void st_dev(void* p, unsigned v) { __hip_atomic_store((unsigned*)p, v, RLXA); }
__device__ __forceinline__ unsigned pk(float lo, float hi) { f32x2 v = {lo, hi}; return __builtin_bit_cast(unsigned, __builtin_convertvector(v, bf16x2_t)); }
__device__ __forceinline__ float blo(unsigned x) { return __uint_as_float(x << 16); }
__device__ __forceinline__ float bhi(unsigned x) { return __uint_as_float(x & 0xffff0000u); }
__device__ __forceinline__ float rb(float x) { return blo(pk(x, x)); }
template <int N> __device__ __forceinline__ void wait_vm() { asm volatile("s_waitcnt vmcnt(%0)" ::"n"(N) : "memory"); }
__device__ __forceinline__ void wait_lgkm0() { asm volatile("s_waitcnt lgkmcnt(0)" ::: "memory"); }
__device__ __forceinline__ void bar() { asm volatile("" ::: "memory"); __builtin_amdgcn_s_barrier(); asm volatile("" ::: "memory"); }
template <int AUX> __device__ __forceinline__ void dma16(const void* g, LAS char* l) {
  __builtin_amdgcn_global_load_lds((const unsigned*)g, (LAS unsigned*)l, 16, 0, AUX);
}
__device__ __forceinline__ bf16x8 lds16(const LAS char* p) { return *(const LAS bf16x8*)p; }
__device__ __forceinline__ bf16x8 ldstr(const LAS char* p0, const LAS char* p1) {
  v4i16 a = __builtin_amdgcn_ds_read_tr16_b64_v4i16((LAS v4i16*)p0), b = __builtin_amdgcn_ds_read_tr16_b64_v4i16((LAS v4i16*)p1);
  return __builtin_shufflevector(a, b, 0, 1, 2, 3, 4, 5, 6, 7);
}
template <int OFF> __device__ __forceinline__ void rd128(bf16x8& d, unsigned a) { asm volatile("ds_read_b128 %0, %1 offset:%2" : "=v"(d) : "v"(a), "n"(OFF)); }
template <int OFF> __device__ __forceinline__ void rd64(u32x2& d, unsigned a) { asm volatile("ds_read_b64 %0, %1 offset:%2" : "=v"(d) : "v"(a), "n"(OFF)); }
template <int OFF> __device__ __forceinline__ void rdtr(u32x2& d, unsigned a) { asm volatile("ds_read_b64_tr_b16 %0, %1 offset:%2" : "=v"(d) : "v"(a), "n"(OFF)); }
__device__ __forceinline__ bf16x8 cat(const u32x2& lo, const u32x2& hi) { return __builtin_bit_cast(bf16x8, u32x4{lo[0], lo[1], hi[0], hi[1]}); }
#define SCHED() __builtin_amdgcn_sched_barrier(0)
#define PIN(x) asm volatile("" : "+v"(x))
__device__ __forceinline__ void wait_vm0_seen() { __builtin_amdgcn_s_waitcnt(0x0F70); }
#define MFMA(a, b, c) __builtin_amdgcn_mfma_f32_16x16x32_bf16(a, b, c, 0, 0, 0)

#define STAMP(i) do {} while (0)

__device__ __forceinline__ void poll_flags(const char* flags, int n, unsigned tag, int lane, char* ws) {
#pragma unroll 1
  for (unsigned spins = 0;; ++spins) {
    unsigned f = tag;
    if (lane < n) f = ld_dev(flags + lane * 4);
    if (__all(f == tag)) break;
    if (spins > (1u << 16)) { if (lane == 0) st_dev(ws + WS_TMO, tag); break; }
    __builtin_amdgcn_s_sleep(2);
  }
}

__device__ __forceinline__ unsigned flag_issue(const char* flags, int n, int lane) {
  unsigned f;
  const char* a = flags + (lane < n ? lane : 0) * 4;
  asm volatile("global_load_dword %0, %1, off sc1" : "=v"(f) : "v"(a) : "memory");
  return f;
}
template <int N> __device__ __forceinline__ void wait_vm_for(unsigned& f) { asm volatile("s_waitcnt vmcnt(%1)" : "+v"(f) : "n"(N) : "memory"); }
template <int N> __device__ __forceinline__ void wait_vm_for(unsigned& f, unsigned& g) { asm volatile("s_waitcnt vmcnt(%2)" : "+v"(f), "+v"(g) : "n"(N) : "memory"); }

__device__ __forceinline__ f32x2 up2(unsigned x) { return f32x2{blo(x), bhi(x)}; }
__device__ __forceinline__ void p_tile(const Params& p, int tile, int lane, unsigned tag) {
  const int cbk = tile >> 5, pbk = tile & 31;
  const int pb = lane & 7, cc = lane >> 3;
  const int d0 = cbk * 64 + cc * 8, sb = pbk * 64 + pb * 8;
  u32x4 kr[11], vr[8], wr[4];
#pragma unroll
  for (int i = 0; i < 4; ++i) wr[i] = *(const u32x4*)(p.sk + d0 * 4 + i * 8);
#pragma unroll
  for (int i = 0; i < 11; ++i) { int s = sb - 3 + i; s = s < 0 ? 0 : s; kr[i] = *(const u32x4*)(p.k + (size_t)s * D + d0); }
#pragma unroll
  for (int i = 0; i < 8; ++i) vr[i] = *(const u32x4*)(p.v + (size_t)(sb + i) * D + d0);
  if (sb == 0) { kr[0] = u32x4{0, 0, 0, 0}; kr[1] = kr[0]; kr[2] = kr[0]; }
  auto rs = __builtin_amdgcn_make_buffer_rsrc(p.ws, 0, WS_END, 0x00020000);
#pragma unroll
  for (int c = 0; c < 4; ++c) {
    const f32x2 w0 = {blo(wr[c][0]), blo(wr[c][2])}, w1 = {bhi(wr[c][0]), bhi(wr[c][2])};
    const f32x2 w2 = {blo(wr[c][1]), blo(wr[c][3])}, w3 = {bhi(wr[c][1]), bhi(wr[c][3])};
    f32x2 kf[11], o[8];
#pragma unroll
    for (int i = 0; i < 11; ++i) kf[i] = up2(kr[i][c]);
#pragma unroll
    for (int i = 0; i < 8; ++i) o[i] = (w3 * kf[i] + w2 * kf[i + 1] + w1 * kf[i + 2] + w0 * kf[i + 3]) * up2(vr[i][c]);
    const u32x4 lo = {pk(o[7].x, o[6].x), pk(o[5].x, o[4].x), pk(o[3].x, o[2].x), pk(o[1].x, o[0].x)};
    const u32x4 hi = {pk(o[7].y, o[6].y), pk(o[5].y, o[4].y), pk(o[3].y, o[2].y), pk(o[1].y, o[0].y)};
    __builtin_amdgcn_raw_buffer_store_b128(lo, rs, WS_KVR + ((d0 + 2 * c) * S + (S - 8 - sb)) * 2, 0, 16);
    __builtin_amdgcn_raw_buffer_store_b128(hi, rs, WS_KVR + ((d0 + 2 * c + 1) * S + (S - 8 - sb)) * 2, 0, 16);
  }
  wait_vm<0>();
  if (lane == 0) st_dev(p.ws + WS_FLAGP + cbk * 128 + pbk * 4, tag);
}

__device__ __forceinline__ void aux_repack(const Params& p, LAS char* lds, int a, int wid, int lane, unsigned tag) {
#pragma unroll 1
  for (int i = wid; i < 120; i += 8) {
    const int kk = i / 40, c_lin = (i % 40) * 64 + lane, row = c_lin / 80, cp = c_lin % 80;
    const int c = (cp & ~15) | ((cp & 15) ^ ((row & 7) << 1));
    dma16<0>(p.wd + (size_t)((3 * a + kk) * 32 + row) * D + c * 8, lds + i * 1024);
  }
  wait_vm0_seen();
  bar();
  auto rs = __builtin_amdgcn_make_buffer_rsrc(p.ws, 0, WS_END, 0x00020000);
  const int g = lane >> 4, qq = (lane & 15) >> 2, pp = lane & 3;
#pragma unroll 5
  for (int ii = 0; ii < 15; ++ii) {
    const int i = wid + 8 * ii;
    const int kk = i / 40, blk = i % 40, cb8 = blk / 5, ct = blk % 5;
    const int c = 10 * cb8 + 2 * ct + (pp >> 1);
    const int r0 = 4 * g + qq, r1 = r0 + 16;
    const int a0 = kk * 40960 + r0 * 1280 + (((c & ~15) | ((c & 15) ^ ((r0 & 7) << 1))) << 4) + 8 * (pp & 1);
    const int a1 = kk * 40960 + r1 * 1280 + (((c & ~15) | ((c & 15) ^ ((r1 & 7) << 1))) << 4) + 8 * (pp & 1);
    const bf16x8 f = ldstr(lds + a0, lds + a1);
    __builtin_amdgcn_raw_buffer_store_b128(__builtin_bit_cast(u32x4, f), rs, WS_WDP + (((cb8 * 48 + 3 * a + kk) * 5 + ct) * 64 + lane) * 16, 0, 16);
  }
  wait_vm<0>();
  bar();
  if (wid == 0 && lane == 0) st_dev(p.ws + WS_FLAGW + a * 4, tag);
}

__device__ __forceinline__ void aux_gate(const Params& p, int row0, int a, int wid, int lane, unsigned tag) {
  const int r4 = lane >> 4, c16 = lane & 15;
  float gw[5][8];
#pragma unroll
  for (int i = 0; i < 5; ++i) {
    const u32x4 t = *(const u32x4*)(p.gw + (c16 + 16 * i) * 8);
#pragma unroll
    for (int j = 0; j < 4; ++j) { gw[i][2 * j] = blo(t[j]); gw[i][2 * j + 1] = bhi(t[j]); }
  }
  u32x4 x[4][5];
#pragma unroll
  for (int it = 0; it < 4; ++it)
#pragma unroll
    for (int i = 0; i < 5; ++i) x[it][i] = *(const u32x4*)(p.q + (size_t)(row0 + wid * 16 + it * 4 + r4) * D + (c16 + 16 * i) * 8);
#pragma unroll
  for (int it = 0; it < 4; ++it) {
    float z = 0.f;
#pragma unroll
    for (int i = 0; i < 5; ++i)
#pragma unroll
      for (int j = 0; j < 4; ++j) z += blo(x[it][i][j]) * gw[i][2 * j] + bhi(x[it][i][j]) * gw[i][2 * j + 1];
    z += __shfl_xor(z, 1); z += __shfl_xor(z, 2); z += __shfl_xor(z, 4); z += __shfl_xor(z, 8);
    z = rb(z);
    const float gt = rb(1.f / (1.f + __expf(-z)));
    if (c16 == 0) st_dev(p.ws + WS_GATE + (row0 + wid * 16 + it * 4 + r4) * 4, __float_as_uint(gt));
  }
  wait_vm<0>();
  bar();
  if (wid == 0 && lane == 0) st_dev(p.ws + WS_FLAGG + a * 4, tag);
}

template <int NX>
__device__ __forceinline__ void g1_issue(const Params& p, LAS char* slot, int ks, int row0, int cb, int lw, unsigned xoff, unsigned woff0, unsigned woff1) {
  const char* xb = (const char*)p.q + (size_t)row0 * (D * 2) + ks * 128;
  const size_t wo = (size_t)(64 * ks + 16 * lw) * (F * 2) + cb * 128;
  dma16<0>((const char*)p.wg + wo + woff0, slot + G1_WG + (2 * lw) * 1024);
  dma16<0>((const char*)p.wu + wo + woff0, slot + G1_WU + (2 * lw) * 1024);
  dma16<0>((const char*)p.wg + wo + 8 * (F * 2) + woff1, slot + G1_WG + (2 * lw + 1) * 1024);
  dma16<0>((const char*)p.wu + wo + 8 * (F * 2) + woff1, slot + G1_WU + (2 * lw + 1) * 1024);
#pragma unroll
  for (int i = 0; i < NX; ++i) dma16<0>(xb + (size_t)(lw + 4 * i) * (8 * D * 2) + xoff, slot + (lw + 4 * i) * 1024);
}
__device__ __forceinline__ unsigned g1_xoff(int lane) { return (lane >> 3) * (D * 2) + (((lane & 7) ^ (lane >> 3)) << 4); }
__device__ __forceinline__ unsigned g1_woff(int lane, int par) { return (lane >> 3) * (F * 2) + (((lane & 7) ^ ((((lane >> 4) & 1) | (par << 1)) << 1)) << 4); }

template <int NX>
__device__ __forceinline__ void g1_loader(const Params& p, LAS char* lds, int row0, int cb, int rot, bool conv, int ch0, int lw, int lane, unsigned tag) {
  constexpr int NP = NX + 4;
  const unsigned xoff = g1_xoff(lane), woff0 = g1_woff(lane, 0), woff1 = g1_woff(lane, 1);
  int ks = rot + 2; ks = ks >= 10 ? ks - 10 : ks;
  int slot = 2 * G1_SLOT;
#pragma unroll 1
  for (int s = 0; s < 8; ++s) {
    wait_vm<NP>();
    bar();
    g1_issue<NX>(p, lds + slot, ks, row0, cb, lw, xoff, woff0, woff1);
    ks = ks == 9 ? 0 : ks + 1;
    slot = slot == 2 * G1_SLOT ? 0 : slot + G1_SLOT;
  }
  wait_vm<NP>();
  bar();
  unsigned fp = tag;
  if (conv) {
    fp = flag_issue(p.ws + WS_FLAGP + (ch0 >> 6) * 128, 32, lane);
    const u16* src = p.hk + (size_t)(ch0 + lw) * S;
#pragma unroll
    for (int a = 0; a < 2; ++a)
#pragma unroll
      for (int j = 0; j < 4; ++j) {
        int e = 8 * (64 * j + lane) + 2 * a - 2; e = e < 0 ? 0 : e;
        dma16<0>(src + e, lds + C_H + lw * C_HSTR + a * C_H2 + 448 + j * 1024);
      }
    wait_vm_for<8>(fp);
    if (!__all(fp == tag)) poll_flags(p.ws + WS_FLAGP + (ch0 >> 6) * 128, 32, tag, lane, p.ws);
  } else wait_vm<0>();
  bar();
  if (conv) {
    const u16* src = (const u16*)(p.ws + WS_KVR) + (size_t)(ch0 + lw) * S;
#pragma unroll
    for (int j = 0; j < 4; ++j) dma16<16>(src + 8 * (64 * j + lane), lds + C_K + lw * C_KSTR + C_K1 + 16 + j * 1024);
#pragma unroll
    for (int j = 0; j < 4; ++j) dma16<16>(src + 1 + 8 * (64 * j + lane), lds + C_K + lw * C_KSTR + 16 + j * 1024);
  }
}

template <int NRT> struct G1Frag { u32x2 wl[4], wh[4]; bf16x8 x[NRT]; };
template <int NRT, int KK, int I>
__device__ __forceinline__ void g1_read1(G1Frag<NRT>& f, unsigned wa0, unsigned wa1, unsigned xa) {
  if constexpr (I == 0) rdtr<KK * 4096>(f.wl[0], wa0);
  else if constexpr (I == 1) rdtr<KK * 4096 + 512>(f.wh[0], wa0);
  else if constexpr (I == 2) rdtr<KK * 4096 + 8192>(f.wl[2], wa0);
  else if constexpr (I == 3) rdtr<KK * 4096 + 8192 + 512>(f.wh[2], wa0);
  else if constexpr (I == 4) rdtr<KK * 4096>(f.wl[1], wa1);
  else if constexpr (I == 5) rdtr<KK * 4096 + 512>(f.wh[1], wa1);
  else if constexpr (I == 6) rdtr<KK * 4096 + 8192>(f.wl[3], wa1);
  else if constexpr (I == 7) rdtr<KK * 4096 + 8192 + 512>(f.wh[3], wa1);
  else rd128<(I - 8) * 2048>(f.x[I - 8], xa);
}
template <int NRT> __device__ __forceinline__ void g1_wait(G1Frag<NRT>& f) {
  asm volatile("s_waitcnt lgkmcnt(0)" : "+v"(f.wl[0]), "+v"(f.wh[0]), "+v"(f.wl[1]), "+v"(f.wh[1]), "+v"(f.wl[2]), "+v"(f.wh[2]), "+v"(f.wl[3]), "+v"(f.wh[3]),
               "+v"(f.x[0]), "+v"(f.x[1]), "+v"(f.x[2]), "+v"(f.x[3])::"memory");
  if constexpr (NRT > 4) asm volatile("" : "+v"(f.x[4]), "+v"(f.x[5]), "+v"(f.x[6]));
}
template <int NRT, int KK, int N>
__device__ __forceinline__ void g1_step(const G1Frag<NRT>& c, G1Frag<NRT>& nx, f32x4 (&ag)[NRT][2], f32x4 (&au)[NRT][2], unsigned wa0, unsigned wa1, unsigned xa) {
  if constexpr (N < 8 + NRT) g1_read1<NRT, KK, N>(nx, wa0, wa1, xa);
  constexpr int m = N / 4, j = N % 4;
  if constexpr (j == 0) ag[m][0] = MFMA(cat(c.wl[0], c.wh[0]), c.x[m], ag[m][0]);
  else if constexpr (j == 1) au[m][0] = MFMA(cat(c.wl[2], c.wh[2]), c.x[m], au[m][0]);
  else if constexpr (j == 2) ag[m][1] = MFMA(cat(c.wl[1], c.wh[1]), c.x[m], ag[m][1]);
  else au[m][1] = MFMA(cat(c.wl[3], c.wh[3]), c.x[m], au[m][1]);
  SCHED();
  if constexpr (N + 1 < 4 * NRT) g1_step<NRT, KK, N + 1>(c, nx, ag, au, wa0, wa1, xa);
}

template <int NRT>
__device__ __forceinline__ void g1_consumer(const Params& p, LAS char* lds, int row0, int cb, int wid, int lane) {
  const int cg = wid & 1, rt0 = (wid >> 1) * NRT, g = lane >> 4, row = lane & 15, qq = row >> 2, pp = lane & 3;
  const int sw = (qq >> 1) | ((g & 1) << 1);
  const unsigned lb = (unsigned)(size_t)lds;
  const unsigned xa0 = lb + rt0 * 2048 + row * 128 + ((g ^ (row & 7)) << 4), xa1 = xa0 ^ 64;
  const unsigned wa0 = lb + G1_WG + (8 * g + qq) * 128 + 32 * ((2 * cg) ^ sw) + 8 * pp;
  const unsigned wa1 = lb + G1_WG + (8 * g + qq) * 128 + 32 * ((2 * cg + 1) ^ sw) + 8 * pp;
  f32x4 ag[NRT][2], au[NRT][2];
  G1Frag<NRT> A, B;
#pragma unroll
  for (int m = 0; m < NRT; ++m) { ag[m][0] = f32x4{0, 0, 0, 0}; ag[m][1] = ag[m][0]; au[m][0] = ag[m][0]; au[m][1] = ag[m][0]; B.x[m] = bf16x8{0, 0, 0, 0, 0, 0, 0, 0}; }
#pragma unroll
  for (int n = 0; n < 4; ++n) { B.wl[n] = u32x2{0, 0}; B.wh[n] = u32x2{0, 0}; }
  int slot = 0;
#pragma unroll 1
  for (int s = 0; s < 10; ++s) {
    bar();
    g1_step<NRT, 0, 0>(B, A, ag, au, wa0 + slot, wa1 + slot, xa0 + slot);
    g1_wait(A);
    g1_step<NRT, 1, 0>(A, B, ag, au, wa0 + slot, wa1 + slot, xa1 + slot);
    g1_wait(B);
    slot = slot == 2 * G1_SLOT ? 0 : slot + G1_SLOT;
  }
#pragma unroll
  for (int m = 0; m < NRT; ++m) {
    ag[m][0] = MFMA(cat(B.wl[0], B.wh[0]), B.x[m], ag[m][0]);
    au[m][0] = MFMA(cat(B.wl[2], B.wh[2]), B.x[m], au[m][0]);
    ag[m][1] = MFMA(cat(B.wl[1], B.wh[1]), B.x[m], ag[m][1]);
    au[m][1] = MFMA(cat(B.wl[3], B.wh[3]), B.x[m], au[m][1]);
  }
  auto rs = __builtin_amdgcn_make_buffer_rsrc(p.ws, 0, WS_END, 0x00020000);
#pragma unroll
  for (int m = 0; m < NRT; ++m) {
    u32x4 w;
#pragma unroll
    for (int n = 0; n < 2; ++n) {
      const unsigned g01 = pk(ag[m][n][0], ag[m][n][1]), g23 = pk(ag[m][n][2], ag[m][n][3]);
      const unsigned u01 = pk(au[m][n][0], au[m][n][1]), u23 = pk(au[m][n][2], au[m][n][3]);
      w[2 * n] = pk(blo(g01) * blo(u01), bhi(g01) * bhi(u01));
      w[2 * n + 1] = pk(blo(g23) * blo(u23), bhi(g23) * bhi(u23));
    }
    const int rtile = (row0 >> 4) + rt0 + m;
    __builtin_amdgcn_raw_buffer_store_b128(w, rs, WS_H + ((rtile * 48 + 2 * cb + cg) * 64 + lane) * 16, 0, 16);
  }
}

struct CSet { bf16x8 a[8]; u32x2 bl[8], bh[8]; };
template <int R, int K> __device__ __forceinline__ void conv_read1(CSet& s, unsigned A0, unsigned B0) {
  if constexpr (K < 8) rd128<64 * R + 512 * K>(s.a[K], A0);
  else if constexpr (((K - 8) & 1) == 0) rd64<64 * R + 512 * (7 - (K - 8) / 2)>(s.bl[(K - 8) / 2], B0);
  else rd64<64 * R + 512 * (7 - (K - 8) / 2) + 8>(s.bh[(K - 8) / 2], B0);
}
template <int R, int K> __device__ __forceinline__ void conv_reads(CSet& s, unsigned A0, unsigned B0) {
  conv_read1<R, K>(s, A0, B0);
  if constexpr (K < 23) conv_reads<R, K + 1>(s, A0, B0);
}
__device__ __forceinline__ void conv_wait(CSet& s) {
  asm volatile("s_waitcnt lgkmcnt(0)" : "+v"(s.a[0]), "+v"(s.a[1]), "+v"(s.a[2]), "+v"(s.a[3]), "+v"(s.a[4]), "+v"(s.a[5]), "+v"(s.a[6]), "+v"(s.a[7]),
               "+v"(s.bl[0]), "+v"(s.bl[1]), "+v"(s.bl[2]), "+v"(s.bl[3]), "+v"(s.bl[4]), "+v"(s.bl[5]), "+v"(s.bl[6]), "+v"(s.bl[7])::"memory");
  asm volatile("" : "+v"(s.bh[0]), "+v"(s.bh[1]), "+v"(s.bh[2]), "+v"(s.bh[3]), "+v"(s.bh[4]), "+v"(s.bh[5]), "+v"(s.bh[6]), "+v"(s.bh[7]));
}
constexpr int conv_pa(int n) { int a = 0; while (n >= 8 - a) { n -= 8 - a; ++a; } return a; }
constexpr int conv_pe(int n) { int a = 0; while (n >= 8 - a) { n -= 8 - a; ++a; } return n; }
template <int R, int N> __device__ __forceinline__ void conv_step(const CSet& c, CSet& nx, f32x4 (&acc)[8], unsigned A0, unsigned B0) {
  if constexpr (R < 7 && N < 24) conv_read1<R + 1, N>(nx, A0, B0);
  constexpr int a = conv_pa(N), e = conv_pe(N);
  acc[a + e] = MFMA(c.a[a], cat(c.bl[e], c.bh[e]), acc[a + e]);
  SCHED();
  if constexpr (N < 35) conv_step<R, N + 1>(c, nx, acc, A0, B0);
}
template <int R> __device__ __forceinline__ void conv_res(const CSet& c, CSet& nx, f32x4 (&acc)[8], unsigned A0, unsigned B0) {
  conv_step<R, 0>(c, nx, acc, A0, B0);
  if constexpr (R < 7) conv_wait(nx);
}

__device__ __forceinline__ void conv_channel(const Params& p, LAS char* lds, int ch, int c, int lane, unsigned tag) {
  LAS char* H = lds + C_H + c * C_HSTR;
  LAS char* K = lds + C_K + c * C_KSTR;
  {
    const u32x4 z = {0, 0, 0, 0};
    if (lane < 56) *(LAS u32x4*)(H + (lane >= 28 ? C_H2 + (lane - 28) * 16 : lane * 16)) = z;
    if (lane < 8) *(LAS u32x4*)(K + (lane >> 2) * C_K1 + (257 + (lane & 3)) * 16) = z;
    if (lane == 0) {
      const u32x4 t = *(LAS u32x4*)(H + C_H2 + 448);
      *(LAS u32x4*)(H + 448) = u32x4{0u, t[0], t[1], t[2]};
      *(LAS u16*)(K + 14) = *(LAS u16*)(K + C_K1 + 16);
      *(LAS u16*)(K + 2055 * 2) = 0;
    }
    wait_lgkm0();
    __builtin_amdgcn_sched_barrier(0);
  }
  const int rc = lane & 15, g = lane >> 4;
  const unsigned A0 = (unsigned)(size_t)H + (rc & 1) * C_H2 + 64 * (rc >> 1) + 16 * g;
  const unsigned B0 = (unsigned)(size_t)K + (rc & 1) * C_K1 + 72 - 8 * (rc >> 1) + 16 * g;
  f32x4 acc[8];
#pragma unroll
  for (int t = 0; t < 8; ++t) acc[t] = f32x4{0, 0, 0, 0};
  CSet X, Y2;
  conv_reads<0, 0>(X, A0, B0);
  conv_wait(X);
  conv_res<0>(X, Y2, acc, A0, B0);
  conv_res<1>(Y2, X, acc, A0, B0);
  conv_res<2>(X, Y2, acc, A0, B0);
  conv_res<3>(Y2, X, acc, A0, B0);
  conv_res<4>(X, Y2, acc, A0, B0);
  conv_res<5>(Y2, X, acc, A0, B0);
  conv_res<6>(X, Y2, acc, A0, B0);
  conv_res<7>(Y2, X, acc, A0, B0);
  asm volatile("" ::: "memory");
  LAS char* Y = H;
  const bool odd = rc & 1;
#pragma unroll
  for (int t = 0; t < 8; ++t) {
    const float s0 = odd ? acc[t][0] : acc[t][2], s1 = odd ? acc[t][1] : acc[t][3];
    const float t0 = __shfl_xor(s0, 1), t1 = __shfl_xor(s1, 1);
    const u32x2 w = odd ? u32x2{pk(t0, acc[t][2]), pk(t1, acc[t][3])} : u32x2{pk(acc[t][0], t0), pk(acc[t][1], t1)};
    *(LAS u32x2*)(Y + 2 * (256 * t + 64 * g + 32 * (rc & 1) + 4 * (rc >> 1))) = w;
  }
  wait_lgkm0();
  __builtin_amdgcn_sched_barrier(0);
  const float dd = blo(p.dv[ch]);
  auto rs = __builtin_amdgcn_make_buffer_rsrc(p.ws, 0, WS_END, 0x00020000);
#pragma unroll
  for (int it = 0; it < 4; ++it) {
    const int pos = 8 * (lane + 64 * it);
    const u32x4 y = *(const LAS u32x4*)(Y + 2 * pos);
    const u32x4 kv = *(const LAS u32x4*)(K + C_K1 + 2 * (2048 - pos));
    u32x4 o;
#pragma unroll
    for (int j = 0; j < 4; ++j) {
      const unsigned kk = kv[3 - j];
      o[j] = pk(blo(y[j]) + rb(bhi(kk) * dd), bhi(y[j]) + rb(blo(kk) * dd));
    }
    __builtin_amdgcn_raw_buffer_store_b128(o, rs, WS_YT + (ch * S + pos) * 2, 0, 16);
  }
  wait_vm<0>();
  if (lane == 0) st_dev(p.ws + WS_FLAGY + (ch / 80) * 512 + (ch % 80) * 4, tag);
}

__device__ __forceinline__ void g2_issue_w(const char* wsrc, LAS char* lds, int i, int lw) {
  LAS char* d = lds + g2_wslot(i % 3);
#pragma unroll
  for (int j = 0; j < 5; ++j) dma16<16>(wsrc + i * 20480 + (lw * 5 + j) * 1024, d + (lw * 5 + j) * 1024);
}
__device__ __forceinline__ void g2_issue_h(const char* hsrc, LAS char* lds, int i, int lw) {
#pragma unroll
  for (int j = 0; j < 4; ++j) dma16<16>(hsrc + i * 4096 + j * 1024, lds + G2_HRING + (i & 3) * 16384 + lw * 4096 + j * 1024);
}
__device__ __forceinline__ void g2_loader(const Params& p, LAS char* lds, int row0, int cb8, int lw, int lane, unsigned tag) {
  const char* hsrc = p.ws + WS_H + (size_t)((row0 >> 4) + lw) * 48 * 1024 + lane * 16;
  const char* wsrc = p.ws + WS_WDP + (size_t)cb8 * 48 * 5120 + lane * 16;
#pragma unroll
  for (int i = 0; i < 4; ++i) g2_issue_h(hsrc, lds, i, lw);
  wait_vm<12>(); bar();
  wait_vm<8>(); bar();
  g2_issue_w(wsrc, lds, 3, lw); g2_issue_h(hsrc, lds, 4, lw);
  unsigned fy0 = tag, fy1 = tag;
#pragma unroll 1
  for (int i = 2; i <= 8; ++i) {
    wait_vm<13>(); bar();
    g2_issue_w(wsrc, lds, i + 2, lw); g2_issue_h(hsrc, lds, i + 3, lw);
    if (i == 6) { fy0 = flag_issue(p.ws + WS_FLAGY + cb8 * 512, 64, lane); fy1 = flag_issue(p.ws + WS_FLAGY + cb8 * 512 + 256, 16, lane); }
  }
  wait_vm_for<13>(fy0, fy1);
  if (!__all(fy0 == tag && fy1 == tag)) { poll_flags(p.ws + WS_FLAGY + cb8 * 512, 64, tag, lane, p.ws); poll_flags(p.ws + WS_FLAGY + cb8 * 512 + 256, 16, tag, lane, p.ws); }
  bar();
  g2_issue_w(wsrc, lds, 11, lw);
  {
    const int rl = lane >> 3, c = (lane & 7) ^ (((rl >> 1) & 3) << 1);
    const char* y = p.ws + WS_YT + ((size_t)(cb8 * 80 + rl) * S + row0 + c * 8) * 2;
#pragma unroll
    for (int j = 0; j < 3; ++j) { const int pc = min(lw * 3 + j, 9); dma16<16>(y + (size_t)pc * 8 * S * 2, lds + G2_YT + pc * 1024); }
  }
  wait_vm<12>(); bar();
  wait_vm<3>(); bar();
  wait_vm<0>(); bar();
}

struct G2Frag { bf16x8 w[2][5], h[2]; };
template <int I, int HALF, int N> __device__ __forceinline__ void g2_read1(G2Frag& f, unsigned wa, unsigned ha) {
  constexpr int H = (I & 3) * 16384 + HALF * 2048, k = N / 6, j = N % 6;
  if constexpr (j == 0) rd128<H + k * 1024>(f.h[k], ha);
  else rd128<(2 * HALF + k) * 5120 + (j - 1) * 1024>(f.w[k][j - 1], wa);
}
template <int I, int HALF, int N> __device__ __forceinline__ void g2_step(const G2Frag& c, G2Frag& nx, f32x4 (&acc)[5], unsigned wa, unsigned ha, bool mul) {
  g2_read1<I, HALF, N>(nx, wa, ha);
  if constexpr (N < 10) { if (mul) acc[N % 5] = MFMA(c.w[N / 5][N % 5], c.h[N / 5], acc[N % 5]); }
  SCHED();
  if constexpr (N < 11) g2_step<I, HALF, N + 1>(c, nx, acc, wa, ha, mul);
}
__device__ __forceinline__ void g2_wait(G2Frag& f) {
  asm volatile("s_waitcnt lgkmcnt(0)" : "+v"(f.h[0]), "+v"(f.h[1]), "+v"(f.w[0][0]), "+v"(f.w[0][1]), "+v"(f.w[0][2]), "+v"(f.w[0][3]), "+v"(f.w[0][4]),
               "+v"(f.w[1][0]), "+v"(f.w[1][1]), "+v"(f.w[1][2]), "+v"(f.w[1][3]), "+v"(f.w[1][4])::"memory");
}
template <int I> __device__ __forceinline__ void g2_iter(G2Frag& X, G2Frag& Y, f32x4 (&acc)[5], unsigned wa, unsigned ha) {
  bar();
  const unsigned w = wa + g2_wslot(I % 3);
  g2_step<I, 0, 0>(Y, X, acc, w, ha, I > 0);
  g2_wait(X);
  g2_step<I, 1, 0>(X, Y, acc, w, ha, true);
  g2_wait(Y);
  if constexpr (I < 11) g2_iter<I + 1>(X, Y, acc, wa, ha);
}

__device__ __forceinline__ void g2_consumer(const Params& p, LAS char* lds, int row0, int cb8, int rt, int lane) {
  const int g = lane >> 4, j = lane & 15;
  const int prow = row0 + rt * 16 + j;
  u32x2 qv[5];
#pragma unroll
  for (int ct = 0; ct < 5; ++ct) qv[ct] = *(const u32x2*)(p.q + (size_t)prow * D + cb8 * 80 + ct * 16 + 4 * g);
  const float gt = __uint_as_float(ld_dev(p.ws + WS_GATE + prow * 4));
  const unsigned lb = (unsigned)(size_t)lds;
  f32x4 acc[5];
#pragma unroll
  for (int ct = 0; ct < 5; ++ct) acc[ct] = f32x4{0, 0, 0, 0};
  G2Frag X, Y;
  g2_iter<0>(X, Y, acc, lb + lane * 16, lb + G2_HRING + rt * 4096 + lane * 16);
#pragma unroll
  for (int n = 0; n < 10; ++n) acc[n % 5] = MFMA(Y.w[n / 5][n % 5], Y.h[n / 5], acc[n % 5]);
  bar();
  const int qq = j >> 2, pp = lane & 3;
  const float omg = rb(1.f - gt);
#pragma unroll
  for (int ct = 0; ct < 5; ++ct) {
    const int yr = 16 * ct + 4 * g + qq, yc = 2 * rt + (pp >> 1);
    const v4i16 yv = __builtin_amdgcn_ds_read_tr16_b64_v4i16((LAS v4i16*)(lds + G2_YT + yr * 128 + ((yc ^ (((yr >> 1) & 3) << 1)) << 4) + 8 * (pp & 1)));
    const u32x2 y2 = __builtin_bit_cast(u32x2, yv);
    const f32x4 b = acc[ct];
    const float a0 = rb(blo(qv[ct][0]) * blo(y2[0])), a1 = rb(bhi(qv[ct][0]) * bhi(y2[0])), a2 = rb(blo(qv[ct][1]) * blo(y2[1])), a3 = rb(bhi(qv[ct][1]) * bhi(y2[1]));
    u32x2 w;
    w[0] = pk(rb(gt * a0) + rb(omg * rb(b[0])), rb(gt * a1) + rb(omg * rb(b[1])));
    w[1] = pk(rb(gt * a2) + rb(omg * rb(b[2])), rb(gt * a3) + rb(omg * rb(b[3])));
    *(u32x2*)(p.out + (size_t)prow * D + cb8 * 80 + ct * 16 + 4 * g) = w;
  }
}

__global__ __launch_bounds__(NTHR, 2) void gm_kernel(Params p) {
  extern __shared__ __attribute__((aligned(16))) char shm[];
  LAS char* lds = (LAS char*)shm;
  const int tid = threadIdx.x, wid = __builtin_amdgcn_readfirstlane(tid >> 6), lane = tid & 63;
  const int b = blockIdx.x, xcd = b & 7, r = b >> 3;
  const unsigned long long T0 = __builtin_amdgcn_s_memrealtime();
  unsigned epoch;
  asm volatile("s_load_dword %0, %1, 0x0" : "=s"(epoch) : "s"(p.ws) : "memory");

  const bool is_g1 = r < 30;
  const int rg = r % 5, cs = r / 5;
  const bool big = is_g1 && rg < 4;
  const int bigidx = cs * 4 + rg;
  const bool conv = big && bigidx < 20;
  const int ch0 = xcd * 80 + bigidx * 4;

  const int row0 = (xcd >> 2) * 1024 + rg * 224, cb = 6 * (xcd & 3) + cs;
  const int rot = (r * 3 + xcd) % 10;
  if (is_g1 && wid >= 4) {
    const unsigned xoff = g1_xoff(lane), woff0 = g1_woff(lane, 0), woff1 = g1_woff(lane, 1);
    const int k1 = rot == 9 ? 0 : rot + 1;
    if (big) { g1_issue<7>(p, lds, rot, row0, cb, wid - 4, xoff, woff0, woff1); g1_issue<7>(p, lds + G1_SLOT, k1, row0, cb, wid - 4, xoff, woff0, woff1); }
    else { g1_issue<4>(p, lds, rot, row0, cb, wid - 4, xoff, woff0, woff1); g1_issue<4>(p, lds + G1_SLOT, k1, row0, cb, wid - 4, xoff, woff0, woff1); }
  }
  asm volatile("s_waitcnt lgkmcnt(0)" : "+s"(epoch)::"memory");
  const unsigned tag = epoch + 1u;
  if (tid == 0) { st_dev(p.ws + WS_STAMP + b * 4, (unsigned)T0); asm volatile("s_waitcnt vmcnt(0)" ::: "memory"); st_dev(p.ws + WS_ARRIVE + b * 4, tag); }
  if (!big) {
    const int pi = xcd * 8 + (is_g1 ? cs : r - 24);
    if (wid < 5) p_tile(p, pi * 5 + wid, lane, tag);
  }
  STAMP(1);
  if (is_g1) {
    if (wid >= 4) {
      if (big) g1_loader<7>(p, lds, row0, cb, rot, conv, ch0, wid - 4, lane, tag);
      else g1_loader<4>(p, lds, row0, cb, rot, false, 0, wid - 4, lane, tag);
    } else {
      if (big) g1_consumer<7>(p, lds, row0, cb, wid, lane);
      else g1_consumer<4>(p, lds, row0, cb, wid, lane);
    }
    STAMP(2);
    wait_vm<0>();
    bar();
    if (tid == 0) st_dev(p.ws + WS_FLAGH + ((xcd >> 2) * 5 + rg) * 128 + cb * 4, tag);
  } else {
    const int a = 2 * xcd + (r - 30);
    aux_repack(p, lds, a, wid, lane, tag);
    aux_gate(p, xcd * 256 + (r - 30) * 128, a, wid, lane, tag);
    STAMP(2);
    if (b == 240 && wid == 0) {
      for (int i = 0; i < 4; ++i) poll_flags(p.ws + WS_ARRIVE + i * 256, 64, tag, lane, p.ws);
      if (lane == 0) st_dev(p.ws + WS_EPOCH, tag);
    }
    bar();
  }
  STAMP(3);
  const int cb8 = r >> 2, orow0 = xcd * 256 + (r & 3) * 64;
  if (wid < 4) {
    if (conv) conv_channel(p, lds, ch0 + wid, wid, lane, tag);
  } else {
    if (wid == 4) poll_flags(p.ws + WS_FLAGW, 16, tag, lane, p.ws);
    if (wid != 4) poll_flags(p.ws + WS_FLAGW, 16, tag, lane, p.ws);
    const char* wsrc = p.ws + WS_WDP + (size_t)cb8 * 48 * 5120 + lane * 16;
#pragma unroll 1
    for (int i = wid - 4; i < 60; i += 4) { const int st = i / 20, pc = i % 20; dma16<16>(wsrc + st * 20480 + pc * 1024, lds + g2_wslot(st) + pc * 1024); }
    if (wid == 4) {
      poll_flags(p.ws + WS_FLAGG, 16, tag, lane, p.ws);
      const int half = orow0 >> 10, loc = orow0 & 1023;
      const int g0 = min(loc / 224, 4), g1 = min((loc + 63) / 224, 4);
      poll_flags(p.ws + WS_FLAGH + (half * 5 + g0) * 128, 24, tag, lane, p.ws);
      if (g1 != g0) poll_flags(p.ws + WS_FLAGH + (half * 5 + g1) * 128, 24, tag, lane, p.ws);
    }
    wait_vm<0>();
  }
  STAMP(4);
  bar();
  STAMP(5);
  if (wid < 4) g2_consumer(p, lds, orow0, cb8, wid, lane);
  else g2_loader(p, lds, orow0, cb8, wid - 4, lane, tag);
  if (b == 240 && wid == 0) {
    unsigned mn = 0xffffffffu, mx = 0;
    const unsigned base = (unsigned)T0 - 100000u;
    for (int i = 0; i < 4; ++i) { unsigned v = ld_dev(p.ws + WS_STAMP + (i * 64 + lane) * 4) - base; mn = min(mn, v); mx = max(mx, v); }
    for (int o = 32; o; o >>= 1) { mn = min(mn, (unsigned)__shfl_xor(mn, o)); mx = max(mx, (unsigned)__shfl_xor(mx, o)); }
    const unsigned until = mn + 5000u + 0u * (mx - mn);
    while ((unsigned)__builtin_amdgcn_s_memrealtime() - base < until) __builtin_amdgcn_s_sleep(1);
  }
}
}

extern "C" void kernel_launch(void* const* d_in, const int* in_sizes, int n_in, void* d_out, int out_size, void* d_ws, size_t ws_size, hipStream_t stream) {
  using namespace gm;
  Params p;
  p.q = (const u16*)d_in[0]; p.k = (const u16*)d_in[1]; p.v = (const u16*)d_in[2]; p.sk = (const u16*)d_in[3];
  p.hk = (const u16*)d_in[4]; p.dv = (const u16*)d_in[5]; p.wg = (const u16*)d_in[7]; p.wu = (const u16*)d_in[8];
  p.wd = (const u16*)d_in[9]; p.gw = (const u16*)d_in[10]; p.out = (u16*)d_out; p.ws = (char*)d_ws;
  (void)hipFuncSetAttribute((const void*)gm_kernel, hipFuncAttributeMaxDynamicSharedMemorySize, LDS_BYTES);
  gm_kernel<<<NWG, NTHR, LDS_BYTES, stream>>>(p);
}
```

```cpp
#include <hip/hip_runtime.h>
#include <stdint.h>

namespace gm {
#define LAS __attribute__((address_space(3)))
typedef unsigned short u16;
typedef short bf16x8 __attribute__((ext_vector_type(8)));
typedef short v4i16 __attribute__((ext_vector_type(4)));
typedef float f32x4 __attribute__((ext_vector_type(4)));
typedef float f32x16 __attribute__((ext_vector_type(16)));
typedef float f32x2 __attribute__((ext_vector_type(2)));
typedef __bf16 bf16x2_t __attribute__((ext_vector_type(2)));
typedef unsigned u32x4 __attribute__((ext_vector_type(4)));
typedef unsigned u32x2 __attribute__((ext_vector_type(2)));

constexpr int S = 2048, D = 640, F = 1536;
constexpr int NWG = 256, NTHR = 512;
constexpr int LDS_BYTES = 163840;

constexpr unsigned WS_EPOCH = 0, WS_ARRIVE = 1024, WS_FLAGP = 4096, WS_FLAGH = 8192, WS_FLAGW = 20480, WS_FLAGG = 15360,
                   WS_FLAGY = 16384, WS_TMO = 24576, WS_GATE = 32768, WS_STAMP = 65536;
constexpr unsigned WS_KVR = 1u << 20, WS_YT = 4u << 20, WS_WDP = 8u << 20, WS_H = 12u << 20, WS_END = 20u << 20;

constexpr int G1_SLOT = 49152, G1_WG = 32768, G1_WU = 40960;
constexpr int RP_IMG = 147456;
constexpr int C_H = 98304, C_HSTR = 9216, C_H2 = 4640;
constexpr int C_K = 64512, C_KSTR = 8448, C_K1 = 4224;
constexpr int L_SIG = 163776;
constexpr int G2_YT = 40960;
__host__ __device__ constexpr int g2_hslot(int i) { return i == 0 ? 40960 : (((i - 1) & 3) < 2 ? 98304 + ((i - 1) & 3) * 16384 : 65536 + (((i - 1) & 3) - 2) * 16384); }
__host__ __device__ constexpr int g2_wslot(int i) { return i < 2 ? i * 20480 : 135168; }

struct Params {
  const u16 *q, *k, *v, *sk, *hk, *dv, *wg, *wu, *wd, *gw;
  u16* out;
  char* ws;
};

#define RLXA __ATOMIC_RELAXED, __HIP_MEMORY_SCOPE_AGENT
__device__ __forceinline__ unsigned ld_dev(const void* p) { return __hip_atomic_load((const unsigned*)p, RLXA); }
__device__ __forceinline__ void st_dev(void* p, unsigned v) { __hip_atomic_store((unsigned*)p, v, RLXA); }
__device__ __forceinline__ unsigned pk(float lo, float hi) { f32x2 v = {lo, hi}; return __builtin_bit_cast(unsigned, __builtin_convertvector(v, bf16x2_t)); }
__device__ __forceinline__ float blo(unsigned x) { return __uint_as_float(x << 16); }
__device__ __forceinline__ float bhi(unsigned x) { return __uint_as_float(x & 0xffff0000u); }
__device__ __forceinline__ float rb(float x) { return blo(pk(x, x)); }
template <int N> __device__ __forceinline__ void wait_vm() { asm volatile("s_waitcnt vmcnt(%0)" ::"n"(N) : "memory"); }
__device__ __forceinline__ void wait_lgkm0() { asm volatile("s_waitcnt lgkmcnt(0)" ::: "memory"); }
__device__ __forceinline__ void bar() { asm volatile("" ::: "memory"); __builtin_amdgcn_s_barrier(); asm volatile("" ::: "memory"); }
template <int AUX> __device__ __forceinline__ void dma16(const void* g, LAS char* l) {
  __builtin_amdgcn_global_load_lds((const unsigned*)g, (LAS unsigned*)l, 16, 0, AUX);
}
typedef __amdgpu_buffer_rsrc_t rsrc_t;
__device__ __forceinline__ rsrc_t mkrs(const void* base, unsigned bytes) { return __builtin_amdgcn_make_buffer_rsrc((void*)base, 0, bytes, 0x00020000); }
template <int AUX> __device__ __forceinline__ void bdma(rsrc_t rs, LAS char* l, unsigned voff, unsigned soff) {
  __builtin_amdgcn_raw_ptr_buffer_load_lds(rs, (LAS void*)l, 16, voff, soff, 0, AUX);
}
__device__ __forceinline__ bf16x8 lds16(const LAS char* p) { return *(const LAS bf16x8*)p; }
__device__ __forceinline__ bf16x8 ldstr(const LAS char* p0, const LAS char* p1) {
  v4i16 a = __builtin_amdgcn_ds_read_tr16_b64_v4i16((LAS v4i16*)p0), b = __builtin_amdgcn_ds_read_tr16_b64_v4i16((LAS v4i16*)p1);
  return __builtin_shufflevector(a, b, 0, 1, 2, 3, 4, 5, 6, 7);
}
template <int OFF> __device__ __forceinline__ void rd128(bf16x8& d, unsigned a) { asm volatile("ds_read_b128 %0, %1 offset:%2" : "=v"(d) : "v"(a), "n"(OFF)); }
template <int OFF> __device__ __forceinline__ void rd64(u32x2& d, unsigned a) { asm volatile("ds_read_b64 %0, %1 offset:%2" : "=v"(d) : "v"(a), "n"(OFF)); }
template <int OFF> __device__ __forceinline__ void rdtr(u32x2& d, unsigned a) { asm volatile("ds_read_b64_tr_b16 %0, %1 offset:%2" : "=v"(d) : "v"(a), "n"(OFF)); }
__device__ __forceinline__ bf16x8 cat(const u32x2& lo, const u32x2& hi) { return __builtin_bit_cast(bf16x8, u32x4{lo[0], lo[1], hi[0], hi[1]}); }
#define SCHED() __builtin_amdgcn_sched_barrier(0)
#define PIN(x) asm volatile("" : "+v"(x))
__device__ __forceinline__ void wait_vm0_seen() { __builtin_amdgcn_s_waitcnt(0x0F70); }
#define MFMA(a, b, c) __builtin_amdgcn_mfma_f32_16x16x32_bf16(a, b, c, 0, 0, 0)
#define MFMA32(a, b, c) __builtin_amdgcn_mfma_f32_32x32x16_bf16(a, b, c, 0, 0, 0)

#define STAMP(i) do {} while (0)
#define STAMP2(i) do {} while (0)

__device__ __forceinline__ void poll_flags(const char* flags, int n, unsigned tag, int lane, char* ws) {
#pragma unroll 1
  for (unsigned spins = 0;; ++spins) {
    unsigned f = tag;
    if (lane < n) f = ld_dev(flags + lane * 4);
    if (__all(f == tag)) break;
    if (spins > (1u << 16)) { if (lane == 0) st_dev(ws + WS_TMO, tag); break; }
    __builtin_amdgcn_s_sleep(1);
  }
}

__device__ __forceinline__ void lds_signal(LAS char* lds, int i, unsigned v) { *(volatile LAS unsigned*)(lds + L_SIG + 4 * i) = v; }
__device__ __forceinline__ void lds_await(LAS char* lds, int i, unsigned v) {
#pragma unroll 1
  for (unsigned n = 0; *(volatile LAS unsigned*)(lds + L_SIG + 4 * i) != v && n < (1u << 22); ++n) __builtin_amdgcn_s_sleep(1);
}
__device__ __forceinline__ void poll_flags2(const char* fa, int na, const char* fb, int nb, unsigned tag, int lane, char* ws) {
#pragma unroll 1
  for (unsigned spins = 0;; ++spins) {
    unsigned f = tag, g = tag;
    if (lane < na) f = ld_dev(fa + lane * 4);
    if (lane < nb) g = ld_dev(fb + lane * 4);
    if (__all(f == tag && g == tag)) break;
    if (spins > (1u << 16)) { if (lane == 0) st_dev(ws + WS_TMO, tag); break; }
    __builtin_amdgcn_s_sleep(1);
  }
}
__device__ __forceinline__ void poll_flags6(const char* f, unsigned tag, int lane, char* ws) {
  const unsigned long long t2 = ((unsigned long long)tag << 32) | tag;
#pragma unroll 1
  for (unsigned spins = 0;; ++spins) {
    const unsigned long long a = __hip_atomic_load((const unsigned long long*)(f + lane * 8), RLXA), b = __hip_atomic_load((const unsigned long long*)(f + 512 + lane * 8), RLXA),
                             c = __hip_atomic_load((const unsigned long long*)(f + 1024 + lane * 8), RLXA);
    if (__all(a == t2 && b == t2 && c == t2)) break;
    if (spins > (1u << 16)) { if (lane == 0) st_dev(ws + WS_TMO, tag); break; }
    __builtin_amdgcn_s_sleep(1);
  }
}
__device__ __forceinline__ unsigned flag_issue(const char* flags, int n, int lane) {
  unsigned f;
  const char* a = flags + (lane < n ? lane : 0) * 4;
  asm volatile("global_load_dword %0, %1, off sc1" : "=v"(f) : "v"(a) : "memory");
  return f;
}
template <int N> __device__ __forceinline__ void wait_vm_for(unsigned& f) { asm volatile("s_waitcnt vmcnt(%1)" : "+v"(f) : "n"(N) : "memory"); }
template <int N> __device__ __forceinline__ void wait_vm_for(unsigned& f, unsigned& g) { asm volatile("s_waitcnt vmcnt(%2)" : "+v"(f), "+v"(g) : "n"(N) : "memory"); }

__device__ __forceinline__ f32x2 up2(unsigned x) { return f32x2{blo(x), bhi(x)}; }
struct PTile { u32x4 kr[11], vr[8], wr[4]; };
__device__ __forceinline__ void p_load(const Params& p, PTile& t, int tile, int lane) {
  const int cbk = tile >> 5, pbk = tile & 31;
  const int pb = lane & 7, cc = lane >> 3;
  const int d0 = cbk * 64 + cc * 8, sb = pbk * 64 + pb * 8;
#pragma unroll
  for (int i = 0; i < 4; ++i) t.wr[i] = *(const u32x4*)(p.sk + d0 * 4 + i * 8);
#pragma unroll
  for (int i = 0; i < 11; ++i) { int s = sb - 3 + i; s = s < 0 ? 0 : s; t.kr[i] = *(const u32x4*)(p.k + (size_t)s * D + d0); }
#pragma unroll
  for (int i = 0; i < 8; ++i) t.vr[i] = *(const u32x4*)(p.v + (size_t)(sb + i) * D + d0);
}
struct PHalf { u32x2 kr[11], vr[8]; u32x4 wr[2]; };
__device__ __forceinline__ void ph_load(const Params& p, PHalf& t, int tile, int hf, int lane) {
  const int cbk = tile >> 5, pbk = tile & 31;
  const int d0 = cbk * 64 + (lane >> 3) * 8 + 4 * hf, sb = pbk * 64 + (lane & 7) * 8;
  t.wr[0] = *(const u32x4*)(p.sk + d0 * 4); t.wr[1] = *(const u32x4*)(p.sk + d0 * 4 + 8);
#pragma unroll
  for (int i = 0; i < 11; ++i) { int s = sb - 3 + i; s = s < 0 ? 0 : s; t.kr[i] = *(const u32x2*)(p.k + (size_t)s * D + d0); }
#pragma unroll
  for (int i = 0; i < 8; ++i) t.vr[i] = *(const u32x2*)(p.v + (size_t)(sb + i) * D + d0);
}
__device__ __forceinline__ void ph_finish(const Params& p, PHalf& t, int tile, int hf, int lane) {
  const int cbk = tile >> 5, pbk = tile & 31;
  const int d0 = cbk * 64 + (lane >> 3) * 8 + 4 * hf, sb = pbk * 64 + (lane & 7) * 8;
  if (sb == 0) { t.kr[0] = u32x2{0, 0}; t.kr[1] = t.kr[0]; t.kr[2] = t.kr[0]; }
  auto rs = __builtin_amdgcn_make_buffer_rsrc(p.ws, 0, WS_END, 0x00020000);
#pragma unroll
  for (int c = 0; c < 2; ++c) {
    const f32x2 w0 = {blo(t.wr[c][0]), blo(t.wr[c][2])}, w1 = {bhi(t.wr[c][0]), bhi(t.wr[c][2])};
    const f32x2 w2 = {blo(t.wr[c][1]), blo(t.wr[c][3])}, w3 = {bhi(t.wr[c][1]), bhi(t.wr[c][3])};
    f32x2 kf[11], o[8];
#pragma unroll
    for (int i = 0; i < 11; ++i) kf[i] = up2(t.kr[i][c]);
#pragma unroll
    for (int i = 0; i < 8; ++i) o[i] = (w3 * kf[i] + w2 * kf[i + 1] + w1 * kf[i + 2] + w0 * kf[i + 3]) * up2(t.vr[i][c]);
    const u32x4 lo = {pk(o[7].x, o[6].x), pk(o[5].x, o[4].x), pk(o[3].x, o[2].x), pk(o[1].x, o[0].x)};
    const u32x4 hi = {pk(o[7].y, o[6].y), pk(o[5].y, o[4].y), pk(o[3].y, o[2].y), pk(o[1].y, o[0].y)};
    __builtin_amdgcn_raw_buffer_store_b128(lo, rs, WS_KVR + ((d0 + 2 * c) * S + (S - 8 - sb)) * 2, 0, 16);
    __builtin_amdgcn_raw_buffer_store_b128(hi, rs, WS_KVR + ((d0 + 2 * c + 1) * S + (S - 8 - sb)) * 2, 0, 16);
  }
}
__device__ __forceinline__ void p_finish(const Params& p, PTile& t, int tile, int lane, unsigned tag) {
  const int cbk = tile >> 5, pbk = tile & 31;
  const int pb = lane & 7, cc = lane >> 3;
  const int d0 = cbk * 64 + cc * 8, sb = pbk * 64 + pb * 8;
  if (sb == 0) { t.kr[0] = u32x4{0, 0, 0, 0}; t.kr[1] = t.kr[0]; t.kr[2] = t.kr[0]; }
  auto rs = __builtin_amdgcn_make_buffer_rsrc(p.ws, 0, WS_END, 0x00020000);
#pragma unroll
  for (int c = 0; c < 4; ++c) {
    const f32x2 w0 = {blo(t.wr[c][0]), blo(t.wr[c][2])}, w1 = {bhi(t.wr[c][0]), bhi(t.wr[c][2])};
    const f32x2 w2 = {blo(t.wr[c][1]), blo(t.wr[c][3])}, w3 = {bhi(t.wr[c][1]), bhi(t.wr[c][3])};
    f32x2 kf[11], o[8];
#pragma unroll
    for (int i = 0; i < 11; ++i) kf[i] = up2(t.kr[i][c]);
#pragma unroll
    for (int i = 0; i < 8; ++i) o[i] = (w3 * kf[i] + w2 * kf[i + 1] + w1 * kf[i + 2] + w0 * kf[i + 3]) * up2(t.vr[i][c]);
    const u32x4 lo = {pk(o[7].x, o[6].x), pk(o[5].x, o[4].x), pk(o[3].x, o[2].x), pk(o[1].x, o[0].x)};
    const u32x4 hi = {pk(o[7].y, o[6].y), pk(o[5].y, o[4].y), pk(o[3].y, o[2].y), pk(o[1].y, o[0].y)};
    __builtin_amdgcn_raw_buffer_store_b128(lo, rs, WS_KVR + ((d0 + 2 * c) * S + (S - 8 - sb)) * 2, 0, 16);
    __builtin_amdgcn_raw_buffer_store_b128(hi, rs, WS_KVR + ((d0 + 2 * c + 1) * S + (S - 8 - sb)) * 2, 0, 16);
  }
  STAMP(11);
  wait_vm<0>();
  if (lane == 0) st_dev(p.ws + WS_FLAGP + cbk * 128 + pbk * 4, tag);
}

__device__ __forceinline__ void repack_issue(const Params& p, LAS char* img, int ks, int cb8, int lane) {
  const rsrc_t rd = mkrs(p.wd, F * D * 2);
#pragma unroll
  for (int j = 0; j < 5; ++j) {
    const int n = 64 * j + lane, row = n / 10, c = n - row * 10;
    bdma<0>(rd, img + j * 1024, row * (D * 2) + c * 16, ks * 32 * (D * 2) + cb8 * 160);
  }
}
struct RpAddr { unsigned a0; unsigned o[5]; };
__device__ __forceinline__ RpAddr repack_addr(LAS char* img, int ks, int cb8, int lane) {
  const int g = lane >> 4, qq = (lane & 15) >> 2, pp = lane & 3;
  RpAddr r;
  r.a0 = (unsigned)(size_t)img + (16 * (g >> 1) + 4 * (g & 1) + qq) * 160 + 8 * pp;
#pragma unroll
  for (int ct = 0; ct < 5; ++ct) r.o[ct] = WS_WDP + (((cb8 * 48 + ks) * 5 + ct) * 64 + lane) * 16;
  return r;
}
__device__ __forceinline__ void repack_store(const Params& p, const RpAddr& r) {
  u32x2 lo[5], hi[5];
  rdtr<0>(lo[0], r.a0); rdtr<1280>(hi[0], r.a0); rdtr<32>(lo[1], r.a0); rdtr<1312>(hi[1], r.a0); rdtr<64>(lo[2], r.a0); rdtr<1344>(hi[2], r.a0);
  rdtr<96>(lo[3], r.a0); rdtr<1376>(hi[3], r.a0); rdtr<128>(lo[4], r.a0); rdtr<1408>(hi[4], r.a0);
  asm volatile("s_waitcnt lgkmcnt(0)" : "+v"(lo[0]), "+v"(lo[1]), "+v"(lo[2]), "+v"(lo[3]), "+v"(lo[4]), "+v"(hi[0]), "+v"(hi[1]), "+v"(hi[2]), "+v"(hi[3]), "+v"(hi[4])::"memory");
  auto rs = __builtin_amdgcn_make_buffer_rsrc(p.ws, 0, WS_END, 0x00020000);
#pragma unroll
  for (int ct = 0; ct < 5; ++ct) __builtin_amdgcn_raw_buffer_store_b128(u32x4{lo[ct][0], lo[ct][1], hi[ct][0], hi[ct][1]}, rs, r.o[ct], 0, 16);
}
__device__ __forceinline__ void repack_finish(const Params& p, LAS char* img, int ks, int cb8, int lane) {
  wait_vm0_seen();
  auto rs = __builtin_amdgcn_make_buffer_rsrc(p.ws, 0, WS_END, 0x00020000);
  const int g = lane >> 4, qq = (lane & 15) >> 2, pp = lane & 3;
  const int r0 = 16 * (g >> 1) + 4 * (g & 1) + qq;
  const LAS char* a0 = img + r0 * 160 + 8 * pp;
#pragma unroll
  for (int ct = 0; ct < 5; ++ct) {
    const bf16x8 f = ldstr(a0 + ct * 32, a0 + 8 * 160 + ct * 32);
    __builtin_amdgcn_raw_buffer_store_b128(__builtin_bit_cast(u32x4, f), rs, WS_WDP + (((cb8 * 48 + ks) * 5 + ct) * 64 + lane) * 16, 0, 16);
  }
}

__device__ __forceinline__ void aux_gate(const Params& p, int row0, int lane) {
  const int r4 = lane >> 4, c16 = lane & 15;
  u32x4 gw[5], x[4][5];
#pragma unroll
  for (int i = 0; i < 5; ++i) {
    gw[i] = *(const u32x4*)(p.gw + (c16 + 16 * i) * 8);
#pragma unroll
    for (int it = 0; it < 4; ++it) x[it][i] = *(const u32x4*)(p.q + (size_t)(row0 + 4 * it + r4) * D + (c16 + 16 * i) * 8);
  }
#pragma unroll
  for (int it = 0; it < 4; ++it) {
    float z = 0.f;
#pragma unroll
    for (int i = 0; i < 5; ++i)
#pragma unroll
      for (int j = 0; j < 4; ++j) z += blo(x[it][i][j]) * blo(gw[i][j]) + bhi(x[it][i][j]) * bhi(gw[i][j]);
    z += __shfl_xor(z, 1); z += __shfl_xor(z, 2); z += __shfl_xor(z, 4); z += __shfl_xor(z, 8);
    z = rb(z);
    const float gt = rb(1.f / (1.f + __expf(-z)));
    if (c16 == 0) st_dev(p.ws + WS_GATE + (row0 + it * 4 + r4) * 4, __float_as_uint(gt));
  }
}

struct G1Off { unsigned x, g, u; };
__device__ __forceinline__ G1Off g1_offsets(int lane, int lw) {
  const int rl = lane >> 3, c = lane & 7;
  return G1Off{(unsigned)(rl * (D * 2) + ((c ^ ((4 * (lw & 1) + (rl >> 1)) & 7)) << 4)),
               (unsigned)(rl * (F * 2) + ((c ^ (((rl >> 1) & 1) << 1)) << 4)),
               (unsigned)(rl * (F * 2) + ((c ^ ((((rl >> 1) & 1) | 2) << 1)) << 4))};
}
template <int NX>
__device__ __forceinline__ void g1_issue(const Params& p, LAS char* slot, int ks, int row0, int cb, int lw, const G1Off& o) {
  const rsrc_t rq = mkrs(p.q, S * D * 2), rg = mkrs(p.wg, D * F * 2), ru = mkrs(p.wu, D * F * 2);
  const unsigned xb = row0 * (D * 2) + ks * 128 + lw * (8 * D * 2);
  const unsigned wo = (64 * ks + 16 * lw) * (F * 2) + cb * 128;
  bdma<0>(rg, slot + G1_WG + (2 * lw) * 1024, o.g, wo);
  bdma<0>(ru, slot + G1_WU + (2 * lw) * 1024, o.u, wo);
  bdma<0>(rg, slot + G1_WG + (2 * lw + 1) * 1024, o.g, wo + 8 * (F * 2));
  bdma<0>(ru, slot + G1_WU + (2 * lw + 1) * 1024, o.u, wo + 8 * (F * 2));
#pragma unroll
  for (int i = 0; i < NX; ++i) bdma<0>(rq, slot + (lw + 4 * i) * 1024, o.x, xb + i * (32 * D * 2));
}

__device__ __forceinline__ void ldsw128(unsigned a, u32x4 v) { asm volatile("ds_write_b128 %0, %1" ::"v"(a), "v"(v) : "memory"); }
__device__ __forceinline__ void conv_issue_k(const Params& p, LAS char* lds, int ch, int lw, int lane) {
  const rsrc_t rw = mkrs(p.ws, WS_END);
  const unsigned so = WS_KVR + ch * (S * 2), vo = lane * 16;
  LAS char* d = lds + C_K + lw * C_KSTR;
#pragma unroll
  for (int j = 0; j < 4; ++j) bdma<16>(rw, d + 16 + C_K1 + j * 1024, vo, so + j * 1024);
#pragma unroll
  for (int j = 0; j < 4; ++j) bdma<16>(rw, d + 16 + j * 1024, vo, so + j * 1024 + 2);
  if (lane < 8) ldsw128((unsigned)(size_t)d + (lane >> 2) * C_K1 + (257 + (lane & 3)) * 16, u32x4{0, 0, 0, 0});
}
template <int NX, bool RP>
__device__ __forceinline__ void g1_loader(const Params& p, LAS char* lds, int row0, int cb, bool conv, int ch0, int lw, int unit, int lane, unsigned tag) {
  constexpr int NP = NX + 4;
  const G1Off off = g1_offsets(lane, lw);
  RpAddr ra;
  if constexpr (RP) ra = repack_addr(lds + RP_IMG + lw * 5120, unit >> 3, unit & 7, lane);
  const int ch = min(ch0 + lw, D - 1);
  const char* touch = (const char*)p.hk + (size_t)ch * (S * 2) + (lane & 31) * 128;
  const char* fpa = p.ws + WS_FLAGP + (min(ch0, D - 4) >> 6) * 128 + (lane & 31) * 4;
  unsigned dummy, fp = tag;
  wait_vm<RP ? NP + 5 : NP>(); bar(); g1_issue<NX>(p, lds + 2 * G1_SLOT, 2, row0, cb, lw, off);
  wait_vm<NP>(); bar(); g1_issue<NX>(p, lds, 3, row0, cb, lw, off);
  wait_vm<NP>(); bar(); g1_issue<NX>(p, lds + G1_SLOT, 4, row0, cb, lw, off);
  wait_vm<NP>(); bar(); g1_issue<NX>(p, lds + 2 * G1_SLOT, 5, row0, cb, lw, off);
  if constexpr (RP) repack_store(p, ra);
  wait_vm<RP ? NP + 5 : NP>(); bar(); g1_issue<NX>(p, lds, 6, row0, cb, lw, off);
  wait_vm<NP>();
  if constexpr (RP) { if (lane == 0) st_dev(p.ws + WS_FLAGW + unit * 4, tag); }
  bar(); g1_issue<NX>(p, lds + G1_SLOT, 7, row0, cb, lw, off);
  asm volatile("global_load_dword %0, %1, off" : "=v"(dummy) : "v"(touch) : "memory");
  wait_vm<RP ? NP + 2 : NP + 1>(); bar(); g1_issue<NX>(p, lds + 2 * G1_SLOT, 8, row0, cb, lw, off);
  wait_vm<NP + 1>(); bar();
  if (conv && lw == 0) asm volatile("global_load_dword %0, %1, off sc1" : "=v"(fp) : "v"(fpa) : "memory");
  g1_issue<NX>(p, lds, 9, row0, cb, lw, off);
  asm volatile("s_waitcnt vmcnt(%2)" : "+v"(fp), "+v"(dummy) : "n"(NP) : "memory");
  if (conv && lw == 0 && __all(fp == tag)) lds_signal(lds, 0, tag);
  wait_lgkm0();
  bar();
  const bool early = conv && *(volatile LAS unsigned*)(lds + L_SIG) == tag;
  if (early) { conv_issue_k(p, lds, ch, lw, lane); wait_vm<8>(); } else wait_vm<0>();
  wait_lgkm0();
  bar();
  if (conv) {
    const rsrc_t rh = mkrs(p.hk, D * S * 2);
    const unsigned so = ch * (S * 2), vo = lane * 16, vz = lane ? lane * 16 - 4 : 0;
    LAS char* d = lds + C_H + lw * C_HSTR;
    bdma<0>(rh, d + 448, vz, so);
#pragma unroll
    for (int j = 1; j < 4; ++j) bdma<0>(rh, d + 448 + j * 1024, vo, so + j * 1024 - 4);
#pragma unroll
    for (int j = 0; j < 4; ++j) bdma<0>(rh, d + 448 + C_H2 + j * 1024, vo, so + j * 1024);
    if (lane < 56) ldsw128((unsigned)(size_t)d + (lane >= 28 ? C_H2 + (lane - 28) * 16 : lane * 16), u32x4{0, 0, 0, 0});
    if (!early) {
      if (lw == 0) { poll_flags(p.ws + WS_FLAGP + (ch0 >> 6) * 128, 32, tag, lane, p.ws); lds_signal(lds, 3, tag); }
      else lds_await(lds, 3, tag);
      conv_issue_k(p, lds, ch, lw, lane);
    }
    wait_lgkm0();
  }
}

template <int NRT> struct G1Frag { u32x2 wl, wh; bf16x8 x[NRT]; };
template <int NRT, int KK, int I>
__device__ __forceinline__ void g1_read1(G1Frag<NRT>& f, unsigned wa, unsigned xa) {
  if constexpr (I == 0) rdtr<KK * 2048>(f.wl, wa);
  else if constexpr (I == 1) rdtr<KK * 2048 + 512>(f.wh, wa);
  else rd128<(I - 2) * 4096>(f.x[I - 2], xa);
}
template <int NRT, int KK, int N, bool LAST>
__device__ __forceinline__ void g1_step(G1Frag<NRT>& c, G1Frag<NRT>& nx, f32x16 (&acc)[NRT], unsigned wa, unsigned xa) {
  constexpr int NR = NRT + 2, PER = LAST ? 3 : 2;
  static_assert(NRT == 8, "");
  if constexpr (N == 0) asm volatile("s_waitcnt lgkmcnt(4)" : "+v"(c.wl), "+v"(c.wh), "+v"(c.x[0]), "+v"(c.x[1]), "+v"(c.x[2]), "+v"(c.x[3]));
  if constexpr (N == 4) asm volatile("s_waitcnt lgkmcnt(%4)" : "+v"(c.x[4]), "+v"(c.x[5]), "+v"(c.x[6]), "+v"(c.x[7]) : "n"(4 * PER < NR ? 4 * PER : NR));
  if constexpr (PER * N < NR) g1_read1<NRT, KK, PER * N>(nx, wa, xa);
  if constexpr (PER * N + 1 < NR) g1_read1<NRT, KK, PER * N + 1>(nx, wa, xa);
  if constexpr (PER == 3 && PER * N + 2 < NR) g1_read1<NRT, KK, PER * N + 2>(nx, wa, xa);
  acc[N] = MFMA32(cat(c.wl, c.wh), c.x[N], acc[N]);
  SCHED();
  if constexpr (N + 1 < NRT) g1_step<NRT, KK, N + 1, LAST>(c, nx, acc, wa, xa);
  else {
    asm volatile("" ::"v"(c.wl), "v"(c.wh), "v"(c.x[0]), "v"(c.x[1]), "v"(c.x[2]), "v"(c.x[3]));
    asm volatile("" ::"v"(c.x[4]), "v"(c.x[5]), "v"(c.x[6]), "v"(c.x[7]));
  }
}

template <int NRT>
__device__ __forceinline__ void g1_consumer(const Params& p, LAS char* lds, int row0, int cb, int w, int lane) {
  const int j = lane & 31, h = lane >> 5, mat = (lane >> 4) & 1, qq = (lane & 15) >> 2, pp = lane & 3;
  const unsigned lb = (unsigned)(size_t)lds;
  const unsigned xl = j * 128 + ((h ^ ((j >> 1) & 7)) << 4);
  const unsigned wa0 = lb + G1_WG + mat * 8192 + (8 * h + qq) * 128 + 32 * (w ^ (((qq >> 1) & 1) | (mat << 1))) + 8 * pp;
  f32x16 acc[NRT];
  G1Frag<NRT> A, B;
#pragma unroll
  for (int m = 0; m < NRT; ++m) {
#pragma unroll
    for (int e = 0; e < 16; ++e) acc[m][e] = 0.f;
    B.x[m] = bf16x8{0, 0, 0, 0, 0, 0, 0, 0};
  }
  B.wl = u32x2{0, 0}; B.wh = u32x2{0, 0};
  int slot = 0;
#pragma unroll 1
  for (int s = 0; s < 10; ++s) {
    bar();
    STAMP2(s);
    const unsigned wa = wa0 + slot, xb = lb + slot;
    g1_step<NRT, 0, 0, false>(B, A, acc, wa, xb + xl);
    g1_step<NRT, 1, 0, false>(A, B, acc, wa, xb + (xl ^ 32));
    g1_step<NRT, 2, 0, false>(B, A, acc, wa, xb + (xl ^ 64));
    g1_step<NRT, 3, 0, true>(A, B, acc, wa, xb + (xl ^ 96));
    asm volatile("s_waitcnt lgkmcnt(0)" : "+v"(B.wl), "+v"(B.wh), "+v"(B.x[0]), "+v"(B.x[1]), "+v"(B.x[2]), "+v"(B.x[3])::"memory");
    asm volatile("" : "+v"(B.x[4]), "+v"(B.x[5]), "+v"(B.x[6]), "+v"(B.x[7]));
    slot = slot == 2 * G1_SLOT ? 0 : slot + G1_SLOT;
  }
#pragma unroll
  for (int m = 0; m < NRT; ++m) acc[m] = MFMA32(cat(B.wl, B.wh), B.x[m], acc[m]);
  auto rs = __builtin_amdgcn_make_buffer_rsrc(p.ws, 0, WS_END, 0x00020000);
#pragma unroll
  for (int m = 0; m < NRT; ++m) {
    u32x4 o;
#pragma unroll
    for (int e = 0; e < 4; ++e) o[e] = pk(acc[m][2 * e] * acc[m][8 + 2 * e], acc[m][2 * e + 1] * acc[m][8 + 2 * e + 1]);
    const int rtile = (row0 >> 4) + 2 * m + (j >> 4);
    __builtin_amdgcn_raw_buffer_store_b128(o, rs, WS_H + ((rtile * 48 + 2 * cb + (w >> 1)) * 64 + (2 * (w & 1) + h) * 16 + (j & 15)) * 16, 0, 16);
  }
  STAMP(2);
}

struct CSet { bf16x8 a[8]; u32x2 bl[8], bh[8]; };
template <int R, int K> __device__ __forceinline__ void conv_read1(CSet& s, unsigned A0, unsigned B0) {
  if constexpr (K < 8) rd128<64 * R + 512 * K>(s.a[K], A0);
  else if constexpr (((K - 8) & 1) == 0) rd64<64 * R + 512 * (7 - (K - 8) / 2)>(s.bl[(K - 8) / 2], B0);
  else rd64<64 * R + 512 * (7 - (K - 8) / 2) + 8>(s.bh[(K - 8) / 2], B0);
}
template <int R, int K> __device__ __forceinline__ void conv_reads(CSet& s, unsigned A0, unsigned B0) {
  conv_read1<R, K>(s, A0, B0);
  if constexpr (K < 23) conv_reads<R, K + 1>(s, A0, B0);
}
__device__ __forceinline__ void conv_wait(CSet& s) {
  asm volatile("s_waitcnt lgkmcnt(0)" : "+v"(s.a[0]), "+v"(s.a[1]), "+v"(s.a[2]), "+v"(s.a[3]), "+v"(s.a[4]), "+v"(s.a[5]), "+v"(s.a[6]), "+v"(s.a[7]),
               "+v"(s.bl[0]), "+v"(s.bl[1]), "+v"(s.bl[2]), "+v"(s.bl[3]), "+v"(s.bl[4]), "+v"(s.bl[5]), "+v"(s.bl[6]), "+v"(s.bl[7])::"memory");
  asm volatile("" : "+v"(s.bh[0]), "+v"(s.bh[1]), "+v"(s.bh[2]), "+v"(s.bh[3]), "+v"(s.bh[4]), "+v"(s.bh[5]), "+v"(s.bh[6]), "+v"(s.bh[7]));
}
constexpr int conv_pa(int n) { int a = 0; while (n >= 8 - a) { n -= 8 - a; ++a; } return a; }
constexpr int conv_pe(int n) { int a = 0; while (n >= 8 - a) { n -= 8 - a; ++a; } return n; }
template <int R, int N> __device__ __forceinline__ void conv_step(const CSet& c, CSet& nx, f32x4 (&acc)[8], unsigned A0, unsigned B0) {
  if constexpr (R < 7 && N < 24) conv_read1<R + 1, N>(nx, A0, B0);
  if constexpr (R == 7 && N < 16) {
    if constexpr (N & 1) rd64<512 * (7 - N / 2)>(nx.bh[N / 2], B0); else rd64<512 * (7 - N / 2) + 64>(nx.bl[N / 2], B0);
  }
  constexpr int a = conv_pa(N), e = conv_pe(N);
  acc[a + e] = MFMA(c.a[a], cat(c.bl[e], c.bh[e]), acc[a + e]);
  SCHED();
  if constexpr (N < 35) conv_step<R, N + 1>(c, nx, acc, A0, B0);
  else {
    asm volatile("" ::"v"(c.a[0]), "v"(c.a[1]), "v"(c.a[2]), "v"(c.a[3]), "v"(c.a[4]), "v"(c.a[5]), "v"(c.a[6]), "v"(c.a[7]),
                 "v"(c.bl[0]), "v"(c.bl[1]), "v"(c.bl[2]), "v"(c.bl[3]), "v"(c.bl[4]), "v"(c.bl[5]), "v"(c.bl[6]), "v"(c.bl[7]));
    asm volatile("" ::"v"(c.bh[0]), "v"(c.bh[1]), "v"(c.bh[2]), "v"(c.bh[3]), "v"(c.bh[4]), "v"(c.bh[5]), "v"(c.bh[6]), "v"(c.bh[7]));
  }
}
template <int R> __device__ __forceinline__ void conv_res(const CSet& c, CSet& nx, f32x4 (&acc)[8], unsigned A0, unsigned B0) {
  conv_step<R, 0>(c, nx, acc, A0, B0);
  conv_wait(nx);
}

__device__ __forceinline__ void conv_channel(const Params& p, LAS char* lds, int ch, int c, int lane, unsigned tag, char* hflag) {
  LAS char* H = lds + C_H + c * C_HSTR;
  LAS char* K = lds + C_K + c * C_KSTR;
  {
    if (lane == 0) {
      const u32x4 t = *(LAS u32x4*)(H + C_H2 + 448);
      *(LAS u32x4*)(H + 448) = u32x4{0u, t[0], t[1], t[2]};
      *(LAS u16*)(K + 14) = *(LAS u16*)(K + C_K1 + 16);
      *(LAS u16*)(K + 2055 * 2) = 0;
    }
    wait_vm<0>();
    if (lane == 0) st_dev(hflag, tag);
    wait_lgkm0();
    __builtin_amdgcn_sched_barrier(0);
  }
  STAMP(7);
  const float dd = blo(p.dv[ch]);
  const int rc = lane & 15, g = lane >> 4;
  const unsigned A0 = (unsigned)(size_t)H + (rc & 1) * C_H2 + 64 * (rc >> 1) + 16 * g;
  const unsigned B0 = (unsigned)(size_t)K + (rc & 1) * C_K1 + 72 - 8 * (rc >> 1) + 16 * g;
  f32x4 acc[8];
#pragma unroll
  for (int t = 0; t < 8; ++t) acc[t] = f32x4{0, 0, 0, 0};
  CSet X, Y2;
  conv_reads<0, 0>(X, A0, B0);
  conv_wait(X);
  conv_res<0>(X, Y2, acc, A0, B0);
  conv_res<1>(Y2, X, acc, A0, B0);
  conv_res<2>(X, Y2, acc, A0, B0);
  conv_res<3>(Y2, X, acc, A0, B0);
  conv_res<4>(X, Y2, acc, A0, B0);
  conv_res<5>(Y2, X, acc, A0, B0);
  conv_res<6>(X, Y2, acc, A0, B0);
  const unsigned KB = (unsigned)(size_t)K + (rc & 1) * C_K1 + 2 * (2052 - 64 * g - 4 * (rc >> 1) - 32 - 1792);
  conv_res<7>(Y2, X, acc, A0, KB);
  lds_signal(lds, 4 + c, tag);
  STAMP(8);
  LAS char* Y = K;
  const bool odd = rc & 1;
#pragma unroll
  for (int t = 0; t < 8; ++t) {
    const float v0 = acc[t][0] + blo(X.bl[t][1]) * dd, v1 = acc[t][1] + blo(X.bl[t][0]) * dd;
    const float v2 = acc[t][2] + blo(X.bh[t][1]) * dd, v3 = acc[t][3] + blo(X.bh[t][0]) * dd;
    const unsigned x01 = pk(v0, v1), x23 = pk(v2, v3);
    const unsigned rcv = (unsigned)__builtin_amdgcn_mov_dpp((int)(odd ? x01 : x23), 0xB1, 0xF, 0xF, true);
    const unsigned a = odd ? rcv : x01, bb = odd ? x23 : rcv;
    *(LAS u32x2*)(Y + 2 * (256 * t + 64 * g + 32 * (rc & 1) + 4 * (rc >> 1))) = u32x2{__builtin_amdgcn_perm(bb, a, 0x05040100u), __builtin_amdgcn_perm(bb, a, 0x07060302u)};
  }
  wait_lgkm0();
  __builtin_amdgcn_sched_barrier(0);
  auto rs = __builtin_amdgcn_make_buffer_rsrc(p.ws, 0, WS_END, 0x00020000);
#pragma unroll
  for (int it = 0; it < 4; ++it) {
    const int pos = 8 * (lane + 64 * it);
    __builtin_amdgcn_raw_buffer_store_b128(*(const LAS u32x4*)(Y + 2 * pos), rs, WS_YT + (ch * S + pos) * 2, 0, 16);
  }
  STAMP(9);
}

__device__ __forceinline__ void g2_issue_w(rsrc_t rw, unsigned wsrc, unsigned vo, LAS char* lds, int i, int lw) {
  LAS char* d = lds + g2_wslot(i % 3);
#pragma unroll
  for (int j = 0; j < 5; ++j) bdma<16>(rw, d + (lw * 5 + j) * 1024, vo, wsrc + i * 20480 + (lw * 5 + j) * 1024);
}
__device__ __forceinline__ void g2_issue_h(rsrc_t rw, unsigned hsrc, unsigned vo, LAS char* lds, int i, int lw) {
#pragma unroll
  for (int j = 0; j < 4; ++j) bdma<16>(rw, lds + g2_hslot(i) + lw * 4096 + j * 1024, vo, hsrc + i * 4096 + j * 1024);
}
__device__ __forceinline__ void g2_loader(const Params& p, LAS char* lds, int row0, int cb8, int lw, int lane, unsigned tag) {
  const rsrc_t rw = mkrs(p.ws, WS_END);
  const unsigned hsrc = WS_H + ((row0 >> 4) + lw) * 48 * 1024, wsrc = WS_WDP + cb8 * 48 * 5120, vo = lane * 16;
  g2_issue_h(rw, hsrc, vo, lds, 3, lw);
  wait_vm<12>(); bar();
  wait_vm<8>(); bar();
  g2_issue_w(rw, wsrc, vo, lds, 3, lw); g2_issue_h(rw, hsrc, vo, lds, 4, lw);
  unsigned fy0 = tag, fy1 = tag;
  const int wid = lw + 4;
  STAMP(10);
#pragma unroll 1
  for (int i = 2; i <= 8; ++i) {
    wait_vm<13>(); bar();
    g2_issue_w(rw, wsrc, vo, lds, i + 2, lw); g2_issue_h(rw, hsrc, vo, lds, i + 3, lw);
    if (i == 6) { fy0 = flag_issue(p.ws + WS_FLAGY + cb8 * 512, 64, lane); fy1 = flag_issue(p.ws + WS_FLAGY + cb8 * 512 + 256, 16, lane); }
  }
  STAMP(11);
  wait_vm_for<13>(fy0, fy1);
  if (!__all(fy0 == tag && fy1 == tag)) { poll_flags(p.ws + WS_FLAGY + cb8 * 512, 64, tag, lane, p.ws); poll_flags(p.ws + WS_FLAGY + cb8 * 512 + 256, 16, tag, lane, p.ws); }
  bar();
  g2_issue_w(rw, wsrc, vo, lds, 11, lw);
  {
    const int rl = lane >> 3, c = (lane & 7) ^ (((rl >> 1) & 3) << 1);
    const unsigned vy = rl * (S * 2) + c * 16, sy = WS_YT + cb8 * 80 * (S * 2) + row0 * 2;
#pragma unroll
    for (int j = 0; j < 3; ++j) { const int pc = min(lw * 3 + j, 9); bdma<16>(rw, lds + G2_YT + pc * 1024, vy, sy + pc * 8 * (S * 2)); }
  }
  STAMP(12);
  wait_vm<12>(); bar();
  wait_vm<3>(); bar();
  wait_vm<0>(); bar();
}

struct G2Frag { bf16x8 w[2][5], h[2]; };
template <int I, int HALF, int N> __device__ __forceinline__ void g2_read1(G2Frag& f, unsigned wa, unsigned ha) {
  constexpr int H = HALF * 2048, k = N / 6, j = N % 6;
  if constexpr (j == 0) rd128<H + k * 1024>(f.h[k], ha);
  else rd128<(2 * HALF + k) * 5120 + (j - 1) * 1024>(f.w[k][j - 1], wa);
}
template <int I, int HALF, int N> __device__ __forceinline__ void g2_step(const G2Frag& c, G2Frag& nx, f32x4 (&acc)[5], unsigned wa, unsigned ha, bool mul) {
  g2_read1<I, HALF, N>(nx, wa, ha);
  if constexpr (N < 10) { if (mul) acc[N % 5] = MFMA(c.w[N / 5][N % 5], c.h[N / 5], acc[N % 5]); }
  SCHED();
  if constexpr (N < 11) g2_step<I, HALF, N + 1>(c, nx, acc, wa, ha, mul);
  else if (mul) asm volatile("" ::"v"(c.h[0]), "v"(c.h[1]), "v"(c.w[0][0]), "v"(c.w[0][1]), "v"(c.w[0][2]), "v"(c.w[0][3]), "v"(c.w[0][4]),
                             "v"(c.w[1][0]), "v"(c.w[1][1]), "v"(c.w[1][2]), "v"(c.w[1][3]), "v"(c.w[1][4]));
}
__device__ __forceinline__ void g2_wait(G2Frag& f) {
  asm volatile("s_waitcnt lgkmcnt(0)" : "+v"(f.h[0]), "+v"(f.h[1]), "+v"(f.w[0][0]), "+v"(f.w[0][1]), "+v"(f.w[0][2]), "+v"(f.w[0][3]), "+v"(f.w[0][4]),
               "+v"(f.w[1][0]), "+v"(f.w[1][1]), "+v"(f.w[1][2]), "+v"(f.w[1][3]), "+v"(f.w[1][4])::"memory");
}
template <int I> __device__ __forceinline__ void g2_iter(G2Frag& X, G2Frag& Y, f32x4 (&acc)[5], unsigned wa, unsigned ha, char* yflag, unsigned tag, int lane) {
  bar();
  if constexpr (I == 1) {
    wait_vm<0>();
    if (yflag && lane == 0) st_dev(yflag, tag);
  }
  const unsigned w = wa + g2_wslot(I % 3), hh = ha + g2_hslot(I);
  g2_step<I, 0, 0>(Y, X, acc, w, hh, I > 0);
  g2_wait(X);
  g2_step<I, 1, 0>(X, Y, acc, w, hh, true);
  g2_wait(Y);
  if constexpr (I < 11) g2_iter<I + 1>(X, Y, acc, wa, ha, yflag, tag, lane);
}

__device__ __forceinline__ void g2_consumer(const Params& p, LAS char* lds, int row0, int cb8, int rt, int lane, unsigned tag, char* yflag) {
  const int g = lane >> 4, j = lane & 15;
  const int prow = row0 + rt * 16 + j;
  u32x2 qv[5];
#pragma unroll
  for (int ct = 0; ct < 5; ++ct) qv[ct] = *(const u32x2*)(p.q + (size_t)prow * D + cb8 * 80 + ct * 16 + 4 * g);
  const unsigned lb = (unsigned)(size_t)lds;
  f32x4 acc[5];
#pragma unroll
  for (int ct = 0; ct < 5; ++ct) acc[ct] = f32x4{0, 0, 0, 0};
  poll_flags(p.ws + WS_FLAGG + (row0 >> 5) * 4, 2, tag, lane, p.ws);
  const float gt = __uint_as_float(ld_dev(p.ws + WS_GATE + prow * 4));
  G2Frag X, Y;
  g2_iter<0>(X, Y, acc, lb + lane * 16, lb + rt * 4096 + lane * 16, yflag, tag, lane);
#pragma unroll
  for (int n = 0; n < 10; ++n) acc[n % 5] = MFMA(Y.w[n / 5][n % 5], Y.h[n / 5], acc[n % 5]);
  bar();
  const int qq = j >> 2, pp = lane & 3;
  const float omg = 1.f - gt;
#pragma unroll
  for (int ct = 0; ct < 5; ++ct) {
    const int yr = 16 * ct + 4 * g + qq, yc = 2 * rt + (pp >> 1);
    const v4i16 yv = __builtin_amdgcn_ds_read_tr16_b64_v4i16((LAS v4i16*)(lds + G2_YT + yr * 128 + ((yc ^ (((yr >> 1) & 3) << 1)) << 4) + 8 * (pp & 1)));
    const u32x2 y2 = __builtin_bit_cast(u32x2, yv);
    const f32x4 b = acc[ct];
    u32x2 w;
    w[0] = pk(gt * (blo(qv[ct][0]) * blo(y2[0])) + omg * b[0], gt * (bhi(qv[ct][0]) * bhi(y2[0])) + omg * b[1]);
    w[1] = pk(gt * (blo(qv[ct][1]) * blo(y2[1])) + omg * b[2], gt * (bhi(qv[ct][1]) * bhi(y2[1])) + omg * b[3]);
    __builtin_amdgcn_raw_buffer_store_b64(w, mkrs(p.out, S * D * 2), (prow * D + cb8 * 80 + ct * 16 + 4 * g) * 2, 0, 16);
  }
}

__global__ __launch_bounds__(NTHR, 2) void gm_kernel(Params p) {
  extern __shared__ __attribute__((aligned(16))) char shm[];
  LAS char* lds = (LAS char*)shm;
  const int tid = threadIdx.x, wid = __builtin_amdgcn_readfirstlane(tid >> 6), lane = tid & 63;
  const int b = blockIdx.x, xcd = b & 7, r = b >> 3;
  STAMP(0);
  unsigned epoch;
  asm volatile("s_load_dword %0, %1, 0x0" : "=s"(epoch) : "s"(p.ws) : "memory");

  const bool is_g1 = r < 24;
  const int rg = r & 3, cs = r >> 2;
  const bool conv = r < 20;
  const int ch0 = xcd * 80 + r * 4;
  const int row0 = (xcd >> 2) * 1024 + rg * 256, cb = 6 * (xcd & 3) + cs;
  if (is_g1 && wid >= 4) {
    const G1Off off = g1_offsets(lane, wid - 4);
    g1_issue<8>(p, lds, 0, row0, cb, wid - 4, off); g1_issue<8>(p, lds + G1_SLOT, 1, row0, cb, wid - 4, off);
    if (wid < 6) { const int unit = (xcd * 24 + r) * 2 + (wid - 4); repack_issue(p, lds + RP_IMG + (wid - 4) * 5120, unit >> 3, unit & 7, lane); }
  }
  asm volatile("s_waitcnt lgkmcnt(0)" : "+s"(epoch)::"memory");
  const unsigned tag = epoch + 1u;
  if (tid == 0) st_dev(p.ws + WS_ARRIVE + b * 4, tag);
  if (tid < 16) lds_signal(lds, tid, tag - 1u);
  STAMP(1);
  char* hflag = p.ws + WS_FLAGH + ((xcd >> 2) * 4 + rg) * 512 + (cb * 4 + wid) * 4;
  if (is_g1) {
    if (wid >= 4) {
      const int unit = (xcd * 24 + r) * 2 + (wid - 4);
      if (wid < 6) g1_loader<8, true>(p, lds, row0, cb, conv, ch0, wid - 4, unit, lane, tag);
      else g1_loader<8, false>(p, lds, row0, cb, conv, ch0, wid - 4, 0, lane, tag);
      STAMP(2);
    }
    else g1_consumer<8>(p, lds, row0, cb, wid, lane);
  } else {
    const int sidx = xcd * 8 + (r - 24);
    if (wid < 4) {
      PTile t;
      p_load(p, t, sidx * 5 + wid, lane);
      p_finish(p, t, sidx * 5 + wid, lane, tag);
    } else if (wid < 6) {
      PHalf t;
      const int tile = sidx * 5 + 4;
      ph_load(p, t, tile, wid - 4, lane);
      ph_finish(p, t, tile, wid - 4, lane);
      wait_vm<0>();
      if (wid == 5) lds_signal(lds, 8, tag);
      else { lds_await(lds, 8, tag); if (lane == 0) st_dev(p.ws + WS_FLAGP + (tile >> 5) * 128 + (tile & 31) * 4, tag); }
    } else {
      aux_gate(p, xcd * 256 + (r - 24) * 32 + (wid - 6) * 16, lane);
      wait_vm<0>();
      if (wid == 7) lds_signal(lds, 9, tag);
      else { lds_await(lds, 9, tag); if (lane == 0) st_dev(p.ws + WS_FLAGG + sidx * 4, tag); }
    }
    STAMP(2);
    if (b == 255 && wid == 1) {
      for (int i = 0; i < 4; ++i) poll_flags(p.ws + WS_ARRIVE + i * 256, 64, tag, lane, p.ws);
      if (lane == 0) st_dev(p.ws + WS_EPOCH, tag);
    }
  }
  STAMP(3);
  const int cb8 = r >> 2, orow0 = xcd * 256 + (r & 3) * 64;
  if (wid >= 4) {
    wait_vm<0>();
    bar();
    const int lw = wid - 4;
    if (lw == 0) { poll_flags6(p.ws + WS_FLAGW, tag, lane, p.ws); lds_signal(lds, 1, tag); }
    else lds_await(lds, 1, tag);
    STAMP(7);
    const rsrc_t rw = mkrs(p.ws, WS_END);
#pragma unroll
    for (int st = 0; st < 3; ++st) g2_issue_w(rw, WS_WDP + cb8 * 48 * 5120, lane * 16, lds, st, lw);
    const char* w = p.ws + WS_WDP + (size_t)cb8 * 48 * 5120 + 3 * 20480 + ((r & 3) * 360 + lw * 90) * 128;
    unsigned d0, d1;
    asm volatile("global_load_dword %0, %1, off sc1" : "=v"(d0) : "v"(w + lane * 128) : "memory");
    asm volatile("global_load_dword %0, %1, off sc1" : "=v"(d1) : "v"(w + (64 + (lane < 26 ? lane : 25)) * 128) : "memory");
    STAMP(8);
    if (lw == 0) {
      const char* f0 = p.ws + WS_FLAGH + (orow0 >> 8) * 512;
      poll_flags2(f0, 64, f0 + 256, 32, tag, lane, p.ws);
      lds_signal(lds, 2, tag);
    } else lds_await(lds, 2, tag);
    asm volatile("s_waitcnt vmcnt(0)" : "+v"(d0), "+v"(d1)::"memory");
    STAMP(9);
    const unsigned hsrc = WS_H + ((orow0 >> 4) + lw) * 48 * 1024;
    g2_issue_h(rw, hsrc, lane * 16, lds, 0, lw);
    if (conv) { lds_await(lds, 4, tag); lds_await(lds, 5, tag); lds_await(lds, 6, tag); lds_await(lds, 7, tag); }
    g2_issue_h(rw, hsrc, lane * 16, lds, 1, lw); g2_issue_h(rw, hsrc, lane * 16, lds, 2, lw);
  } else {
    bar();
    if (conv) conv_channel(p, lds, ch0 + wid, wid, lane, tag, hflag);
    else if (is_g1) { wait_vm<0>(); if (lane == 0) st_dev(hflag, tag); }
  }
  STAMP(4);
  bar();
  STAMP(5);
  if (wid < 4) g2_consumer(p, lds, orow0, cb8, wid, lane, tag, conv ? p.ws + WS_FLAGY + ((ch0 + wid) / 80) * 512 + ((ch0 + wid) % 80) * 4 : nullptr);
  else g2_loader(p, lds, orow0, cb8, wid - 4, lane, tag);
  STAMP(6);
}
}

extern "C" void kernel_launch(void* const* d_in, const int* in_sizes, int n_in, void* d_out, int out_size, void* d_ws, size_t ws_size, hipStream_t stream) {
  using namespace gm;
  Params p;
  p.q = (const u16*)d_in[0]; p.k = (const u16*)d_in[1]; p.v = (const u16*)d_in[2]; p.sk = (const u16*)d_in[3];
  p.hk = (const u16*)d_in[4]; p.dv = (const u16*)d_in[5]; p.wg = (const u16*)d_in[7]; p.wu = (const u16*)d_in[8];
  p.wd = (const u16*)d_in[9]; p.gw = (const u16*)d_in[10]; p.out = (u16*)d_out; p.ws = (char*)d_ws;
  (void)hipFuncSetAttribute((const void*)gm_kernel, hipFuncAttributeMaxDynamicSharedMemorySize, LDS_BYTES);
  gm_kernel<<<NWG, NTHR, LDS_BYTES, stream>>>(p);
}
```

```cpp
#include <hip/hip_runtime.h>
#include <stdint.h>

namespace gm {
#define LAS __attribute__((address_space(3)))
typedef unsigned short u16;
typedef short bf16x8 __attribute__((ext_vector_type(8)));
typedef short v4i16 __attribute__((ext_vector_type(4)));
typedef float f32x4 __attribute__((ext_vector_type(4)));
typedef float f32x16 __attribute__((ext_vector_type(16)));
typedef float f32x2 __attribute__((ext_vector_type(2)));
typedef __bf16 bf16x2_t __attribute__((ext_vector_type(2)));
typedef unsigned u32x4 __attribute__((ext_vector_type(4)));
typedef unsigned u32x2 __attribute__((ext_vector_type(2)));

constexpr int S = 2048, D = 640, F = 1536;
constexpr int NWG = 256, NTHR = 512;
constexpr int LDS_BYTES = 163840;

constexpr unsigned WS_EPOCH = 0, WS_ARRIVE = 1024, WS_FLAGP = 4096, WS_FLAGH = 8192, WS_FLAGW = 20480, WS_FLAGG = 15360,
                   WS_FLAGY = 16384, WS_TMO = 24576, WS_GATE = 32768, WS_STAMP = 65536;
constexpr unsigned WS_KVR = 1u << 20, WS_YT = 4u << 20, WS_WDP = 8u << 20, WS_H = 12u << 20, WS_END = 20u << 20;

constexpr int G1_SLOT = 49152, G1_WG = 32768, G1_WU = 40960;
constexpr int RP_IMG = 147456;
constexpr int C_H = 98304, C_HSTR = 9216, C_H2 = 4640;
constexpr int C_K = 64512, C_KSTR = 8448, C_K1 = 4224;
constexpr int L_SIG = 163776;
constexpr int G2_YT = 40960;
__host__ __device__ constexpr int g2_hslot(int i) { return i == 0 ? 40960 : (((i - 1) & 3) < 2 ? 98304 + ((i - 1) & 3) * 16384 : 65536 + (((i - 1) & 3) - 2) * 16384); }
__host__ __device__ constexpr int g2_wslot(int i) { return i < 2 ? i * 20480 : 135168; }

struct Params {
  const u16 *q, *k, *v, *sk, *hk, *dv, *wg, *wu, *wd, *gw;
  u16* out;
  char* ws;
};

#define RLXA __ATOMIC_RELAXED, __HIP_MEMORY_SCOPE_AGENT
__device__ __forceinline__ unsigned ld_dev(const void* p) { return __hip_atomic_load((const unsigned*)p, RLXA); }
__device__ __forceinline__ void st_dev(void* p, unsigned v) { __hip_atomic_store((unsigned*)p, v, RLXA); }
__device__ __forceinline__ unsigned pk(float lo, float hi) { f32x2 v = {lo, hi}; return __builtin_bit_cast(unsigned, __builtin_convertvector(v, bf16x2_t)); }
__device__ __forceinline__ float blo(unsigned x) { return __uint_as_float(x << 16); }
__device__ __forceinline__ float bhi(unsigned x) { return __uint_as_float(x & 0xffff0000u); }
__device__ __forceinline__ float rb(float x) { return blo(pk(x, x)); }
template <int N> __device__ __forceinline__ void wait_vm() { asm volatile("s_waitcnt vmcnt(%0)" ::"n"(N) : "memory"); }
__device__ __forceinline__ void wait_lgkm0() { asm volatile("s_waitcnt lgkmcnt(0)" ::: "memory"); }
__device__ __forceinline__ void bar() { asm volatile("" ::: "memory"); __builtin_amdgcn_s_barrier(); asm volatile("" ::: "memory"); }
template <int AUX> __device__ __forceinline__ void dma16(const void* g, LAS char* l) {
  __builtin_amdgcn_global_load_lds((const unsigned*)g, (LAS unsigned*)l, 16, 0, AUX);
}
typedef __amdgpu_buffer_rsrc_t rsrc_t;
__device__ __forceinline__ rsrc_t mkrs(const void* base, unsigned bytes) { return __builtin_amdgcn_make_buffer_rsrc((void*)base, 0, bytes, 0x00020000); }
template <int AUX> __device__ __forceinline__ void bdma(rsrc_t rs, LAS char* l, unsigned voff, unsigned soff) {
  __builtin_amdgcn_raw_ptr_buffer_load_lds(rs, (LAS void*)l, 16, voff, soff, 0, AUX);
}
__device__ __forceinline__ bf16x8 lds16(const LAS char* p) { return *(const LAS bf16x8*)p; }
__device__ __forceinline__ bf16x8 ldstr(const LAS char* p0, const LAS char* p1) {
  v4i16 a = __builtin_amdgcn_ds_read_tr16_b64_v4i16((LAS v4i16*)p0), b = __builtin_amdgcn_ds_read_tr16_b64_v4i16((LAS v4i16*)p1);
  return __builtin_shufflevector(a, b, 0, 1, 2, 3, 4, 5, 6, 7);
}
template <int OFF> __device__ __forceinline__ void rd128(bf16x8& d, unsigned a) { asm volatile("ds_read_b128 %0, %1 offset:%2" : "=v"(d) : "v"(a), "n"(OFF)); }
template <int OFF> __device__ __forceinline__ void rd64(u32x2& d, unsigned a) { asm volatile("ds_read_b64 %0, %1 offset:%2" : "=v"(d) : "v"(a), "n"(OFF)); }
template <int OFF> __device__ __forceinline__ void rdtr(u32x2& d, unsigned a) { asm volatile("ds_read_b64_tr_b16 %0, %1 offset:%2" : "=v"(d) : "v"(a), "n"(OFF)); }
__device__ __forceinline__ bf16x8 cat(const u32x2& lo, const u32x2& hi) { return __builtin_bit_cast(bf16x8, u32x4{lo[0], lo[1], hi[0], hi[1]}); }
#define SCHED() __builtin_amdgcn_sched_barrier(0)
#define PIN(x) asm volatile("" : "+v"(x))
__device__ __forceinline__ void wait_vm0_seen() { __builtin_amdgcn_s_waitcnt(0x0F70); }
#define MFMA(a, b, c) __builtin_amdgcn_mfma_f32_16x16x32_bf16(a, b, c, 0, 0, 0)
#define MFMA32(a, b, c) __builtin_amdgcn_mfma_f32_32x32x16_bf16(a, b, c, 0, 0, 0)

#define STAMP(i) do {} while (0)
#define STAMP2(i) do {} while (0)

__device__ __forceinline__ void poll_flags(const char* flags, int n, unsigned tag, int lane, char* ws) {
#pragma unroll 1
  for (unsigned spins = 0;; ++spins) {
    unsigned f = tag;
    if (lane < n) f = ld_dev(flags + lane * 4);
    if (__all(f == tag)) break;
    if (spins > (1u << 16)) { if (lane == 0) st_dev(ws + WS_TMO, tag); break; }
    __builtin_amdgcn_s_sleep(1);
  }
}

__device__ __forceinline__ void lds_signal(LAS char* lds, int i, unsigned v) { *(volatile LAS unsigned*)(lds + L_SIG + 4 * i) = v; }
__device__ __forceinline__ void lds_await(LAS char* lds, int i, unsigned v) {
#pragma unroll 1
  for (unsigned n = 0; *(volatile LAS unsigned*)(lds + L_SIG + 4 * i) != v && n < (1u << 22); ++n) __builtin_amdgcn_s_sleep(1);
}
__device__ __forceinline__ void poll_flags2(const char* fa, int na, const char* fb, int nb, unsigned tag, int lane, char* ws) {
#pragma unroll 1
  for (unsigned spins = 0;; ++spins) {
    unsigned f = tag, g = tag;
    if (lane < na) f = ld_dev(fa + lane * 4);
    if (lane < nb) g = ld_dev(fb + lane * 4);
    if (__all(f == tag && g == tag)) break;
    if (spins > (1u << 16)) { if (lane == 0) st_dev(ws + WS_TMO, tag); break; }
    __builtin_amdgcn_s_sleep(1);
  }
}
__device__ __forceinline__ void poll_flags6(const char* f, unsigned tag, int lane, char* ws) {
  const unsigned long long t2 = ((unsigned long long)tag << 32) | tag;
#pragma unroll 1
  for (unsigned spins = 0;; ++spins) {
    const unsigned long long a = __hip_atomic_load((const unsigned long long*)(f + lane * 8), RLXA), b = __hip_atomic_load((const unsigned long long*)(f + 512 + lane * 8), RLXA),
                             c = __hip_atomic_load((const unsigned long long*)(f + 1024 + lane * 8), RLXA);
    if (__all(a == t2 && b == t2 && c == t2)) break;
    if (spins > (1u << 16)) { if (lane == 0) st_dev(ws + WS_TMO, tag); break; }
    __builtin_amdgcn_s_sleep(1);
  }
}
__device__ __forceinline__ unsigned flag_issue(const char* flags, int n, int lane) {
  unsigned f;
  const char* a = flags + (lane < n ? lane : 0) * 4;
  asm volatile("global_load_dword %0, %1, off sc1" : "=v"(f) : "v"(a) : "memory");
  return f;
}
template <int N> __device__ __forceinline__ void wait_vm_for(unsigned& f) { asm volatile("s_waitcnt vmcnt(%1)" : "+v"(f) : "n"(N) : "memory"); }
template <int N> __device__ __forceinline__ void wait_vm_for(unsigned& f, unsigned& g) { asm volatile("s_waitcnt vmcnt(%2)" : "+v"(f), "+v"(g) : "n"(N) : "memory"); }

__device__ __forceinline__ f32x2 up2(unsigned x) { return f32x2{blo(x), bhi(x)}; }
struct PTile { u32x4 kr[11], vr[8], wr[4]; };
__device__ __forceinline__ void p_load(const Params& p, PTile& t, int tile, int lane) {
  const int cbk = tile >> 5, pbk = tile & 31;
  const int pb = lane & 7, cc = lane >> 3;
  const int d0 = cbk * 64 + cc * 8, sb = pbk * 64 + pb * 8;
#pragma unroll
  for (int i = 0; i < 4; ++i) t.wr[i] = *(const u32x4*)(p.sk + d0 * 4 + i * 8);
#pragma unroll
  for (int i = 0; i < 11; ++i) { int s = sb - 3 + i; s = s < 0 ? 0 : s; t.kr[i] = *(const u32x4*)(p.k + (size_t)s * D + d0); }
#pragma unroll
  for (int i = 0; i < 8; ++i) t.vr[i] = *(const u32x4*)(p.v + (size_t)(sb + i) * D + d0);
}
struct PHalf { u32x2 kr[11], vr[8]; u32x4 wr[2]; };
__device__ __forceinline__ void ph_load(const Params& p, PHalf& t, int tile, int hf, int lane) {
  const int cbk = tile >> 5, pbk = tile & 31;
  const int d0 = cbk * 64 + (lane >> 3) * 8 + 4 * hf, sb = pbk * 64 + (lane & 7) * 8;
  t.wr[0] = *(const u32x4*)(p.sk + d0 * 4); t.wr[1] = *(const u32x4*)(p.sk + d0 * 4 + 8);
#pragma unroll
  for (int i = 0; i < 11; ++i) { int s = sb - 3 + i; s = s < 0 ? 0 : s; t.kr[i] = *(const u32x2*)(p.k + (size_t)s * D + d0); }
#pragma unroll
  for (int i = 0; i < 8; ++i) t.vr[i] = *(const u32x2*)(p.v + (size_t)(sb + i) * D + d0);
}
__device__ __forceinline__ void ph_finish(const Params& p, PHalf& t, int tile, int hf, int lane) {
  const int cbk = tile >> 5, pbk = tile & 31;
  const int d0 = cbk * 64 + (lane >> 3) * 8 + 4 * hf, sb = pbk * 64 + (lane & 7) * 8;
  if (sb == 0) { t.kr[0] = u32x2{0, 0}; t.kr[1] = t.kr[0]; t.kr[2] = t.kr[0]; }
  auto rs = __builtin_amdgcn_make_buffer_rsrc(p.ws, 0, WS_END, 0x00020000);
#pragma unroll
  for (int c = 0; c < 2; ++c) {
    const f32x2 w0 = {blo(t.wr[c][0]), blo(t.wr[c][2])}, w1 = {bhi(t.wr[c][0]), bhi(t.wr[c][2])};
    const f32x2 w2 = {blo(t.wr[c][1]), blo(t.wr[c][3])}, w3 = {bhi(t.wr[c][1]), bhi(t.wr[c][3])};
    f32x2 kf[11], o[8];
#pragma unroll
    for (int i = 0; i < 11; ++i) kf[i] = up2(t.kr[i][c]);
#pragma unroll
    for (int i = 0; i < 8; ++i) o[i] = (w3 * kf[i] + w2 * kf[i + 1] + w1 * kf[i + 2] + w0 * kf[i + 3]) * up2(t.vr[i][c]);
    const u32x4 lo = {pk(o[7].x, o[6].x), pk(o[5].x, o[4].x), pk(o[3].x, o[2].x), pk(o[1].x, o[0].x)};
    const u32x4 hi = {pk(o[7].y, o[6].y), pk(o[5].y, o[4].y), pk(o[3].y, o[2].y), pk(o[1].y, o[0].y)};
    __builtin_amdgcn_raw_buffer_store_b128(lo, rs, WS_KVR + ((d0 + 2 * c) * S + (S - 8 - sb)) * 2, 0, 16);
    __builtin_amdgcn_raw_buffer_store_b128(hi, rs, WS_KVR + ((d0 + 2 * c + 1) * S + (S - 8 - sb)) * 2, 0, 16);
  }
}
__device__ __forceinline__ void p_finish(const Params& p, PTile& t, int tile, int lane, unsigned tag) {
  const int cbk = tile >> 5, pbk = tile & 31;
  const int pb = lane & 7, cc = lane >> 3;
  const int d0 = cbk * 64 + cc * 8, sb = pbk * 64 + pb * 8;
  if (sb == 0) { t.kr[0] = u32x4{0, 0, 0, 0}; t.kr[1] = t.kr[0]; t.kr[2] = t.kr[0]; }
  auto rs = __builtin_amdgcn_make_buffer_rsrc(p.ws, 0, WS_END, 0x00020000);
#pragma unroll
  for (int c = 0; c < 4; ++c) {
    const f32x2 w0 = {blo(t.wr[c][0]), blo(t.wr[c][2])}, w1 = {bhi(t.wr[c][0]), bhi(t.wr[c][2])};
    const f32x2 w2 = {blo(t.wr[c][1]), blo(t.wr[c][3])}, w3 = {bhi(t.wr[c][1]), bhi(t.wr[c][3])};
    f32x2 kf[11], o[8];
#pragma unroll
    for (int i = 0; i < 11; ++i) kf[i] = up2(t.kr[i][c]);
#pragma unroll
    for (int i = 0; i < 8; ++i) o[i] = (w3 * kf[i] + w2 * kf[i + 1] + w1 * kf[i + 2] + w0 * kf[i + 3]) * up2(t.vr[i][c]);
    const u32x4 lo = {pk(o[7].x, o[6].x), pk(o[5].x, o[4].x), pk(o[3].x, o[2].x), pk(o[1].x, o[0].x)};
    const u32x4 hi = {pk(o[7].y, o[6].y), pk(o[5].y, o[4].y), pk(o[3].y, o[2].y), pk(o[1].y, o[0].y)};
    __builtin_amdgcn_raw_buffer_store_b128(lo, rs, WS_KVR + ((d0 + 2 * c) * S + (S - 8 - sb)) * 2, 0, 16);
    __builtin_amdgcn_raw_buffer_store_b128(hi, rs, WS_KVR + ((d0 + 2 * c + 1) * S + (S - 8 - sb)) * 2, 0, 16);
  }
  STAMP(11);
  wait_vm<0>();
  if (lane == 0) st_dev(p.ws + WS_FLAGP + cbk * 128 + pbk * 4, tag);
}

__device__ __forceinline__ void repack_issue(const Params& p, LAS char* img, int ks, int cb8, int lane) {
  const rsrc_t rd = mkrs(p.wd, F * D * 2);
#pragma unroll
  for (int j = 0; j < 5; ++j) {
    const int n = 64 * j + lane, row = n / 10, c = n - row * 10;
    bdma<0>(rd, img + j * 1024, row * (D * 2) + c * 16, ks * 32 * (D * 2) + cb8 * 160);
  }
}
struct RpAddr { unsigned a0; unsigned o[5]; };
__device__ __forceinline__ RpAddr repack_addr(LAS char* img, int ks, int cb8, int lane) {
  const int g = lane >> 4, qq = (lane & 15) >> 2, pp = lane & 3;
  RpAddr r;
  r.a0 = (unsigned)(size_t)img + (16 * (g >> 1) + 4 * (g & 1) + qq) * 160 + 8 * pp;
#pragma unroll
  for (int ct = 0; ct < 5; ++ct) r.o[ct] = WS_WDP + (((cb8 * 48 + ks) * 5 + ct) * 64 + lane) * 16;
  return r;
}
__device__ __forceinline__ void repack_store(const Params& p, const RpAddr& r) {
  u32x2 lo[5], hi[5];
  rdtr<0>(lo[0], r.a0); rdtr<1280>(hi[0], r.a0); rdtr<32>(lo[1], r.a0); rdtr<1312>(hi[1], r.a0); rdtr<64>(lo[2], r.a0); rdtr<1344>(hi[2], r.a0);
  rdtr<96>(lo[3], r.a0); rdtr<1376>(hi[3], r.a0); rdtr<128>(lo[4], r.a0); rdtr<1408>(hi[4], r.a0);
  asm volatile("s_waitcnt lgkmcnt(0)" : "+v"(lo[0]), "+v"(lo[1]), "+v"(lo[2]), "+v"(lo[3]), "+v"(lo[4]), "+v"(hi[0]), "+v"(hi[1]), "+v"(hi[2]), "+v"(hi[3]), "+v"(hi[4])::"memory");
  auto rs = __builtin_amdgcn_make_buffer_rsrc(p.ws, 0, WS_END, 0x00020000);
#pragma unroll
  for (int ct = 0; ct < 5; ++ct) __builtin_amdgcn_raw_buffer_store_b128(u32x4{lo[ct][0], lo[ct][1], hi[ct][0], hi[ct][1]}, rs, r.o[ct], 0, 16);
}
__device__ __forceinline__ void repack_finish(const Params& p, LAS char* img, int ks, int cb8, int lane) {
  wait_vm0_seen();
  auto rs = __builtin_amdgcn_make_buffer_rsrc(p.ws, 0, WS_END, 0x00020000);
  const int g = lane >> 4, qq = (lane & 15) >> 2, pp = lane & 3;
  const int r0 = 16 * (g >> 1) + 4 * (g & 1) + qq;
  const LAS char* a0 = img + r0 * 160 + 8 * pp;
#pragma unroll
  for (int ct = 0; ct < 5; ++ct) {
    const bf16x8 f = ldstr(a0 + ct * 32, a0 + 8 * 160 + ct * 32);
    __builtin_amdgcn_raw_buffer_store_b128(__builtin_bit_cast(u32x4, f), rs, WS_WDP + (((cb8 * 48 + ks) * 5 + ct) * 64 + lane) * 16, 0, 16);
  }
}

__device__ __forceinline__ void aux_gate(const Params& p, int row0, int lane) {
  const int r4 = lane >> 4, c16 = lane & 15;
  u32x4 gw[5], x[4][5];
#pragma unroll
  for (int i = 0; i < 5; ++i) {
    gw[i] = *(const u32x4*)(p.gw + (c16 + 16 * i) * 8);
#pragma unroll
    for (int it = 0; it < 4; ++it) x[it][i] = *(const u32x4*)(p.q + (size_t)(row0 + 4 * it + r4) * D + (c16 + 16 * i) * 8);
  }
#pragma unroll
  for (int it = 0; it < 4; ++it) {
    float z = 0.f;
#pragma unroll
    for (int i = 0; i < 5; ++i)
#pragma unroll
      for (int j = 0; j < 4; ++j) z += blo(x[it][i][j]) * blo(gw[i][j]) + bhi(x[it][i][j]) * bhi(gw[i][j]);
    z += __shfl_xor(z, 1); z += __shfl_xor(z, 2); z += __shfl_xor(z, 4); z += __shfl_xor(z, 8);
    z = rb(z);
    const float gt = rb(1.f / (1.f + __expf(-z)));
    if (c16 == 0) st_dev(p.ws + WS_GATE + (row0 + it * 4 + r4) * 4, __float_as_uint(gt));
  }
}

struct G1Off { unsigned x, g, u; };
__device__ __forceinline__ G1Off g1_offsets(int lane, int lw) {
  const int rl = lane >> 3, c = lane & 7;
  return G1Off{(unsigned)(rl * (D * 2) + ((c ^ ((4 * (lw & 1) + (rl >> 1)) & 7)) << 4)),
               (unsigned)(rl * (F * 2) + ((c ^ (((rl >> 1) & 1) << 1)) << 4)),
               (unsigned)(rl * (F * 2) + ((c ^ ((((rl >> 1) & 1) | 2) << 1)) << 4))};
}
template <int NX>
__device__ __forceinline__ void g1_issue(const Params& p, LAS char* slot, int ks, int row0, int cb, int lw, const G1Off& o) {
  const rsrc_t rq = mkrs(p.q, S * D * 2), rg = mkrs(p.wg, D * F * 2), ru = mkrs(p.wu, D * F * 2);
  const unsigned xb = row0 * (D * 2) + ks * 128 + lw * (8 * D * 2);
  const unsigned wo = (64 * ks + 16 * lw) * (F * 2) + cb * 128;
  bdma<0>(rg, slot + G1_WG + (2 * lw) * 1024, o.g, wo);
  bdma<0>(ru, slot + G1_WU + (2 * lw) * 1024, o.u, wo);
  bdma<0>(rg, slot + G1_WG + (2 * lw + 1) * 1024, o.g, wo + 8 * (F * 2));
  bdma<0>(ru, slot + G1_WU + (2 * lw + 1) * 1024, o.u, wo + 8 * (F * 2));
#pragma unroll
  for (int i = 0; i < NX; ++i) bdma<0>(rq, slot + (lw + 4 * i) * 1024, o.x, xb + i * (32 * D * 2));
}

__device__ __forceinline__ void ldsw128(unsigned a, u32x4 v) { asm volatile("ds_write_b128 %0, %1" ::"v"(a), "v"(v) : "memory"); }
__device__ __forceinline__ void conv_issue_k(const Params& p, LAS char* lds, int ch, int lw, int lane) {
  const rsrc_t rw = mkrs(p.ws, WS_END);
  const unsigned so = WS_KVR + ch * (S * 2), vo = lane * 16;
  LAS char* d = lds + C_K + lw * C_KSTR;
#pragma unroll
  for (int j = 0; j < 4; ++j) bdma<16>(rw, d + 16 + C_K1 + j * 1024, vo, so + j * 1024);
#pragma unroll
  for (int j = 0; j < 4; ++j) bdma<16>(rw, d + 16 + j * 1024, vo, so + j * 1024 + 2);
  if (lane < 8) ldsw128((unsigned)(size_t)d + (lane >> 2) * C_K1 + (257 + (lane & 3)) * 16, u32x4{0, 0, 0, 0});
}
template <int NX, bool RP>
__device__ __forceinline__ void g1_loader(const Params& p, LAS char* lds, int row0, int cb, bool conv, int ch0, int lw, int unit, int lane, unsigned tag) {
  constexpr int NP = NX + 4;
  const G1Off off = g1_offsets(lane, lw);
  RpAddr ra;
  if constexpr (RP) ra = repack_addr(lds + RP_IMG + lw * 5120, unit >> 3, unit & 7, lane);
  const int ch = min(ch0 + lw, D - 1);
  const char* touch = (const char*)p.hk + (size_t)ch * (S * 2) + (lane & 31) * 128;
  const char* fpa = p.ws + WS_FLAGP + (min(ch0, D - 4) >> 6) * 128 + (lane & 31) * 4;
  unsigned dummy, fp = tag;
  wait_vm<RP ? NP + 5 : NP>(); bar(); g1_issue<NX>(p, lds + 2 * G1_SLOT, 2, row0, cb, lw, off);
  wait_vm<NP>(); bar(); g1_issue<NX>(p, lds, 3, row0, cb, lw, off);
  wait_vm<NP>(); bar(); g1_issue<NX>(p, lds + G1_SLOT, 4, row0, cb, lw, off);
  wait_vm<NP>(); bar(); g1_issue<NX>(p, lds + 2 * G1_SLOT, 5, row0, cb, lw, off);
  if constexpr (RP) repack_store(p, ra);
  wait_vm<RP ? NP + 5 : NP>(); bar(); g1_issue<NX>(p, lds, 6, row0, cb, lw, off);
  wait_vm<NP>();
  if constexpr (RP) { if (lane == 0) st_dev(p.ws + WS_FLAGW + unit * 4, tag); }
  bar(); g1_issue<NX>(p, lds + G1_SLOT, 7, row0, cb, lw, off);
  asm volatile("global_load_dword %0, %1, off" : "=v"(dummy) : "v"(touch) : "memory");
  wait_vm<RP ? NP + 2 : NP + 1>(); bar(); g1_issue<NX>(p, lds + 2 * G1_SLOT, 8, row0, cb, lw, off);
  wait_vm<NP + 1>(); bar();
  if (conv && lw == 0) asm volatile("global_load_dword %0, %1, off sc1" : "=v"(fp) : "v"(fpa) : "memory");
  g1_issue<NX>(p, lds, 9, row0, cb, lw, off);
  asm volatile("s_waitcnt vmcnt(%2)" : "+v"(fp), "+v"(dummy) : "n"(NP) : "memory");
  if (conv && lw == 0 && __all(fp == tag)) lds_signal(lds, 0, tag);
  wait_lgkm0();
  bar();
  const bool early = conv && *(volatile LAS unsigned*)(lds + L_SIG) == tag;
  if (early) { conv_issue_k(p, lds, ch, lw, lane); wait_vm<8>(); } else wait_vm<0>();
  wait_lgkm0();
  bar();
  if (conv) {
    const rsrc_t rh = mkrs(p.hk, D * S * 2);
    const unsigned so = ch * (S * 2), vo = lane * 16, vz = lane ? lane * 16 - 4 : 0;
    LAS char* d = lds + C_H + lw * C_HSTR;
    bdma<0>(rh, d + 448, vz, so);
#pragma unroll
    for (int j = 1; j < 4; ++j) bdma<0>(rh, d + 448 + j * 1024, vo, so + j * 1024 - 4);
#pragma unroll
    for (int j = 0; j < 4; ++j) bdma<0>(rh, d + 448 + C_H2 + j * 1024, vo, so + j * 1024);
    if (lane < 56) ldsw128((unsigned)(size_t)d + (lane >= 28 ? C_H2 + (lane - 28) * 16 : lane * 16), u32x4{0, 0, 0, 0});
    if (!early) {
      if (lw == 0) { poll_flags(p.ws + WS_FLAGP + (ch0 >> 6) * 128, 32, tag, lane, p.ws); lds_signal(lds, 3, tag); }
      else lds_await(lds, 3, tag);
      conv_issue_k(p, lds, ch, lw, lane);
    }
    wait_lgkm0();
  }
}

template <int NRT> struct G1Frag { u32x2 wl, wh; bf16x8 x[NRT]; };
template <int NRT, int KK, int I>
__device__ __forceinline__ void g1_read1(G1Frag<NRT>& f, unsigned wa, unsigned xa) {
  if constexpr (I == 0) rdtr<KK * 2048>(f.wl, wa);
  else if constexpr (I == 1) rdtr<KK * 2048 + 512>(f.wh, wa);
  else rd128<(I - 2) * 4096>(f.x[I - 2], xa);
}
template <int NRT, int KK, int N, bool LAST>
__device__ __forceinline__ void g1_step(G1Frag<NRT>& c, G1Frag<NRT>& nx, f32x16 (&acc)[NRT], unsigned wa, unsigned xa) {
  constexpr int NR = NRT + 2, PER = LAST ? 3 : 2;
  static_assert(NRT == 8, "");
  if constexpr (N == 0) asm volatile("s_waitcnt lgkmcnt(4)" : "+v"(c.wl), "+v"(c.wh), "+v"(c.x[0]), "+v"(c.x[1]), "+v"(c.x[2]), "+v"(c.x[3]));
  if constexpr (N == 4) asm volatile("s_waitcnt lgkmcnt(%4)" : "+v"(c.x[4]), "+v"(c.x[5]), "+v"(c.x[6]), "+v"(c.x[7]) : "n"(4 * PER < NR ? 4 * PER : NR));
  if constexpr (PER * N < NR) g1_read1<NRT, KK, PER * N>(nx, wa, xa);
  if constexpr (PER * N + 1 < NR) g1_read1<NRT, KK, PER * N + 1>(nx, wa, xa);
  if constexpr (PER == 3 && PER * N + 2 < NR) g1_read1<NRT, KK, PER * N + 2>(nx, wa, xa);
  acc[N] = MFMA32(cat(c.wl, c.wh), c.x[N], acc[N]);
  SCHED();
  if constexpr (N + 1 < NRT) g1_step<NRT, KK, N + 1, LAST>(c, nx, acc, wa, xa);
  else {
    asm volatile("" ::"v"(c.wl), "v"(c.wh), "v"(c.x[0]), "v"(c.x[1]), "v"(c.x[2]), "v"(c.x[3]));
    asm volatile("" ::"v"(c.x[4]), "v"(c.x[5]), "v"(c.x[6]), "v"(c.x[7]));
  }
}

template <int NRT>
__device__ __forceinline__ void g1_consumer(const Params& p, LAS char* lds, int row0, int cb, int w, int lane) {
  const int j = lane & 31, h = lane >> 5, mat = (lane >> 4) & 1, qq = (lane & 15) >> 2, pp = lane & 3;
  const unsigned lb = (unsigned)(size_t)lds;
  const unsigned xl = j * 128 + ((h ^ ((j >> 1) & 7)) << 4);
  const unsigned wa0 = lb + G1_WG + mat * 8192 + (8 * h + qq) * 128 + 32 * (w ^ (((qq >> 1) & 1) | (mat << 1))) + 8 * pp;
  f32x16 acc[NRT];
  G1Frag<NRT> A, B;
#pragma unroll
  for (int m = 0; m < NRT; ++m) {
#pragma unroll
    for (int e = 0; e < 16; ++e) acc[m][e] = 0.f;
    B.x[m] = bf16x8{0, 0, 0, 0, 0, 0, 0, 0};
  }
  B.wl = u32x2{0, 0}; B.wh = u32x2{0, 0};
  int slot = 0;
#pragma unroll 1
  for (int s = 0; s < 10; ++s) {
    bar();
    STAMP2(s);
    const unsigned wa = wa0 + slot, xb = lb + slot;
    g1_step<NRT, 0, 0, false>(B, A, acc, wa, xb + xl);
    g1_step<NRT, 1, 0, false>(A, B, acc, wa, xb + (xl ^ 32));
    g1_step<NRT, 2, 0, false>(B, A, acc, wa, xb + (xl ^ 64));
    g1_step<NRT, 3, 0, true>(A, B, acc, wa, xb + (xl ^ 96));
    asm volatile("s_waitcnt lgkmcnt(0)" : "+v"(B.wl), "+v"(B.wh), "+v"(B.x[0]), "+v"(B.x[1]), "+v"(B.x[2]), "+v"(B.x[3])::"memory");
    asm volatile("" : "+v"(B.x[4]), "+v"(B.x[5]), "+v"(B.x[6]), "+v"(B.x[7]));
    slot = slot == 2 * G1_SLOT ? 0 : slot + G1_SLOT;
  }
#pragma unroll
  for (int m = 0; m < NRT; ++m) acc[m] = MFMA32(cat(B.wl, B.wh), B.x[m], acc[m]);
  auto rs = __builtin_amdgcn_make_buffer_rsrc(p.ws, 0, WS_END, 0x00020000);
#pragma unroll
  for (int m = 0; m < NRT; ++m) {
    u32x4 o;
#pragma unroll
    for (int e = 0; e < 4; ++e) o[e] = pk(acc[m][2 * e] * acc[m][8 + 2 * e], acc[m][2 * e + 1] * acc[m][8 + 2 * e + 1]);
    const int rtile = (row0 >> 4) + 2 * m + (j >> 4);
    __builtin_amdgcn_raw_buffer_store_b128(o, rs, WS_H + ((rtile * 48 + 2 * cb + (w >> 1)) * 64 + (2 * (w & 1) + h) * 16 + (j & 15)) * 16, 0, 16);
  }
  STAMP(2);
}

struct CSet { bf16x8 a[8]; u32x2 bl[8], bh[8]; };
template <int R, int K> __device__ __forceinline__ void conv_read1(CSet& s, unsigned A0, unsigned B0) {
  if constexpr (K < 8) rd128<64 * R + 512 * K>(s.a[K], A0);
  else if constexpr (((K - 8) & 1) == 0) rd64<64 * R + 512 * (7 - (K - 8) / 2)>(s.bl[(K - 8) / 2], B0);
  else rd64<64 * R + 512 * (7 - (K - 8) / 2) + 8>(s.bh[(K - 8) / 2], B0);
}
template <int R, int K> __device__ __forceinline__ void conv_reads(CSet& s, unsigned A0, unsigned B0) {
  conv_read1<R, K>(s, A0, B0);
  if constexpr (K < 23) conv_reads<R, K + 1>(s, A0, B0);
}
__device__ __forceinline__ void conv_wait(CSet& s) {
  asm volatile("s_waitcnt lgkmcnt(0)" : "+v"(s.a[0]), "+v"(s.a[1]), "+v"(s.a[2]), "+v"(s.a[3]), "+v"(s.a[4]), "+v"(s.a[5]), "+v"(s.a[6]), "+v"(s.a[7]),
               "+v"(s.bl[0]), "+v"(s.bl[1]), "+v"(s.bl[2]), "+v"(s.bl[3]), "+v"(s.bl[4]), "+v"(s.bl[5]), "+v"(s.bl[6]), "+v"(s.bl[7])::"memory");
  asm volatile("" : "+v"(s.bh[0]), "+v"(s.bh[1]), "+v"(s.bh[2]), "+v"(s.bh[3]), "+v"(s.bh[4]), "+v"(s.bh[5]), "+v"(s.bh[6]), "+v"(s.bh[7]));
}
constexpr int conv_pa(int n) { int a = 0; while (n >= 8 - a) { n -= 8 - a; ++a; } return a; }
constexpr int conv_pe(int n) { int a = 0; while (n >= 8 - a) { n -= 8 - a; ++a; } return n; }
template <int R, int N> __device__ __forceinline__ void conv_step(const CSet& c, CSet& nx, f32x4 (&acc)[8], unsigned A0, unsigned B0) {
  if constexpr (R < 7 && N < 24) conv_read1<R + 1, N>(nx, A0, B0);
  if constexpr (R == 7 && N < 16) {
    if constexpr (N & 1) rd64<512 * (7 - N / 2)>(nx.bh[N / 2], B0); else rd64<512 * (7 - N / 2) + 64>(nx.bl[N / 2], B0);
  }
  constexpr int a = conv_pa(N), e = conv_pe(N);
  acc[a + e] = MFMA(c.a[a], cat(c.bl[e], c.bh[e]), acc[a + e]);
  SCHED();
  if constexpr (N < 35) conv_step<R, N + 1>(c, nx, acc, A0, B0);
  else {
    asm volatile("" ::"v"(c.a[0]), "v"(c.a[1]), "v"(c.a[2]), "v"(c.a[3]), "v"(c.a[4]), "v"(c.a[5]), "v"(c.a[6]), "v"(c.a[7]),
                 "v"(c.bl[0]), "v"(c.bl[1]), "v"(c.bl[2]), "v"(c.bl[3]), "v"(c.bl[4]), "v"(c.bl[5]), "v"(c.bl[6]), "v"(c.bl[7]));
    asm volatile("" ::"v"(c.bh[0]), "v"(c.bh[1]), "v"(c.bh[2]), "v"(c.bh[3]), "v"(c.bh[4]), "v"(c.bh[5]), "v"(c.bh[6]), "v"(c.bh[7]));
  }
}
template <int R> __device__ __forceinline__ void conv_res(const CSet& c, CSet& nx, f32x4 (&acc)[8], unsigned A0, unsigned B0) {
  conv_step<R, 0>(c, nx, acc, A0, B0);
  conv_wait(nx);
}

__device__ __forceinline__ void conv_channel(const Params& p, LAS char* lds, int ch, int c, int lane, unsigned tag, char* hflag) {
  LAS char* H = lds + C_H + c * C_HSTR;
  LAS char* K = lds + C_K + c * C_KSTR;
  {
    if (lane == 0) {
      const u32x4 t = *(LAS u32x4*)(H + C_H2 + 448);
      *(LAS u32x4*)(H + 448) = u32x4{0u, t[0], t[1], t[2]};
      *(LAS u16*)(K + 14) = *(LAS u16*)(K + C_K1 + 16);
      *(LAS u16*)(K + 2055 * 2) = 0;
    }
    wait_vm<0>();
    if (lane == 0) st_dev(hflag, tag);
    wait_lgkm0();
    __builtin_amdgcn_sched_barrier(0);
  }
  STAMP(7);
  const float dd = blo(p.dv[ch]);
  const int rc = lane & 15, g = lane >> 4;
  const unsigned A0 = (unsigned)(size_t)H + (rc & 1) * C_H2 + 64 * (rc >> 1) + 16 * g;
  const unsigned B0 = (unsigned)(size_t)K + (rc & 1) * C_K1 + 72 - 8 * (rc >> 1) + 16 * g;
  f32x4 acc[8];
#pragma unroll
  for (int t = 0; t < 8; ++t) acc[t] = f32x4{0, 0, 0, 0};
  CSet X, Y2;
  conv_reads<0, 0>(X, A0, B0);
  conv_wait(X);
  conv_res<0>(X, Y2, acc, A0, B0);
  conv_res<1>(Y2, X, acc, A0, B0);
  conv_res<2>(X, Y2, acc, A0, B0);
  conv_res<3>(Y2, X, acc, A0, B0);
  conv_res<4>(X, Y2, acc, A0, B0);
  conv_res<5>(Y2, X, acc, A0, B0);
  conv_res<6>(X, Y2, acc, A0, B0);
  const unsigned KB = (unsigned)(size_t)K + (rc & 1) * C_K1 + 2 * (2052 - 64 * g - 4 * (rc >> 1) - 32 - 1792);
  conv_res<7>(Y2, X, acc, A0, KB);
  lds_signal(lds, 4 + c, tag);
  STAMP(8);
  LAS char* Y = K;
  const bool odd = rc & 1;
#pragma unroll
  for (int t = 0; t < 8; ++t) {
    const float v0 = acc[t][0] + blo(X.bl[t][1]) * dd, v1 = acc[t][1] + blo(X.bl[t][0]) * dd;
    const float v2 = acc[t][2] + blo(X.bh[t][1]) * dd, v3 = acc[t][3] + blo(X.bh[t][0]) * dd;
    const unsigned x01 = pk(v0, v1), x23 = pk(v2, v3);
    const unsigned rcv = (unsigned)__builtin_amdgcn_mov_dpp((int)(odd ? x01 : x23), 0xB1, 0xF, 0xF, true);
    const unsigned a = odd ? rcv : x01, bb = odd ? x23 : rcv;
    *(LAS u32x2*)(Y + 2 * (256 * t + 64 * g + 32 * (rc & 1) + 4 * (rc >> 1))) = u32x2{__builtin_amdgcn_perm(bb, a, 0x05040100u), __builtin_amdgcn_perm(bb, a, 0x07060302u)};
  }
  wait_lgkm0();
  __builtin_amdgcn_sched_barrier(0);
  auto rs = __builtin_amdgcn_make_buffer_rsrc(p.ws, 0, WS_END, 0x00020000);
#pragma unroll
  for (int it = 0; it < 4; ++it) {
    const int pos = 8 * (lane + 64 * it);
    __builtin_amdgcn_raw_buffer_store_b128(*(const LAS u32x4*)(Y + 2 * pos), rs, WS_YT + (ch * S + pos) * 2, 0, 16);
  }
  STAMP(9);
}

__device__ __forceinline__ void g2_issue_w(rsrc_t rw, unsigned wsrc, unsigned vo, LAS char* lds, int i, int lw) {
  LAS char* d = lds + g2_wslot(i % 3);
#pragma unroll
  for (int j = 0; j < 5; ++j) bdma<16>(rw, d + (lw * 5 + j) * 1024, vo, wsrc + i * 20480 + (lw * 5 + j) * 1024);
}
__device__ __forceinline__ void g2_issue_h(rsrc_t rw, unsigned hsrc, unsigned vo, LAS char* lds, int i, int lw) {
#pragma unroll
  for (int j = 0; j < 4; ++j) bdma<16>(rw, lds + g2_hslot(i) + lw * 4096 + j * 1024, vo, hsrc + i * 4096 + j * 1024);
}
__device__ __forceinline__ void g2_loader(const Params& p, LAS char* lds, int row0, int cb8, int lw, int lane, unsigned tag) {
  const rsrc_t rw = mkrs(p.ws, WS_END);
  const unsigned hsrc = WS_H + ((row0 >> 4) + lw) * 48 * 1024, wsrc = WS_WDP + cb8 * 48 * 5120, vo = lane * 16;
  g2_issue_h(rw, hsrc, vo, lds, 3, lw);
  wait_vm<12>(); bar();
  wait_vm<8>(); bar();
  g2_issue_w(rw, wsrc, vo, lds, 3, lw); g2_issue_h(rw, hsrc, vo, lds, 4, lw);
  unsigned fy0 = tag, fy1 = tag;
  const int wid = lw + 4;
  STAMP(10);
#pragma unroll 1
  for (int i = 2; i <= 8; ++i) {
    wait_vm<13>(); bar();
    g2_issue_w(rw, wsrc, vo, lds, i + 2, lw); g2_issue_h(rw, hsrc, vo, lds, i + 3, lw);
    if (i == 6) { fy0 = flag_issue(p.ws + WS_FLAGY + cb8 * 512, 64, lane); fy1 = flag_issue(p.ws + WS_FLAGY + cb8 * 512 + 256, 16, lane); }
  }
  STAMP(11);
  wait_vm_for<13>(fy0, fy1);
  if (!__all(fy0 == tag && fy1 == tag)) { poll_flags(p.ws + WS_FLAGY + cb8 * 512, 64, tag, lane, p.ws); poll_flags(p.ws + WS_FLAGY + cb8 * 512 + 256, 16, tag, lane, p.ws); }
  bar();
  g2_issue_w(rw, wsrc, vo, lds, 11, lw);
  {
    const int rl = lane >> 3, c = (lane & 7) ^ (((rl >> 1) & 3) << 1);
    const unsigned vy = rl * (S * 2) + c * 16, sy = WS_YT + cb8 * 80 * (S * 2) + row0 * 2;
#pragma unroll
    for (int j = 0; j < 3; ++j) { const int pc = min(lw * 3 + j, 9); bdma<16>(rw, lds + G2_YT + pc * 1024, vy, sy + pc * 8 * (S * 2)); }
  }
  STAMP(12);
  wait_vm<12>(); bar();
  wait_vm<3>(); bar();
  wait_vm<0>(); bar();
}

struct G2Frag { bf16x8 w[2][5], h[2]; };
template <int I, int HALF, int N> __device__ __forceinline__ void g2_read1(G2Frag& f, unsigned wa, unsigned ha) {
  constexpr int H = HALF * 2048, k = N / 6, j = N % 6;
  if constexpr (j == 0) rd128<H + k * 1024>(f.h[k], ha);
  else rd128<(2 * HALF + k) * 5120 + (j - 1) * 1024>(f.w[k][j - 1], wa);
}
template <int I, int HALF, int N> __device__ __forceinline__ void g2_step(const G2Frag& c, G2Frag& nx, f32x4 (&acc)[5], unsigned wa, unsigned ha, bool mul) {
  g2_read1<I, HALF, N>(nx, wa, ha);
  if constexpr (N < 10) { if (mul) acc[N % 5] = MFMA(c.w[N / 5][N % 5], c.h[N / 5], acc[N % 5]); }
  SCHED();
  if constexpr (N < 11) g2_step<I, HALF, N + 1>(c, nx, acc, wa, ha, mul);
  else if (mul) asm volatile("" ::"v"(c.h[0]), "v"(c.h[1]), "v"(c.w[0][0]), "v"(c.w[0][1]), "v"(c.w[0][2]), "v"(c.w[0][3]), "v"(c.w[0][4]),
                             "v"(c.w[1][0]), "v"(c.w[1][1]), "v"(c.w[1][2]), "v"(c.w[1][3]), "v"(c.w[1][4]));
}
__device__ __forceinline__ void g2_wait(G2Frag& f) {
  asm volatile("s_waitcnt lgkmcnt(0)" : "+v"(f.h[0]), "+v"(f.h[1]), "+v"(f.w[0][0]), "+v"(f.w[0][1]), "+v"(f.w[0][2]), "+v"(f.w[0][3]), "+v"(f.w[0][4]),
               "+v"(f.w[1][0]), "+v"(f.w[1][1]), "+v"(f.w[1][2]), "+v"(f.w[1][3]), "+v"(f.w[1][4])::"memory");
}
template <int I> __device__ __forceinline__ void g2_iter(G2Frag& X, G2Frag& Y, f32x4 (&acc)[5], unsigned wa, unsigned ha, char* yflag, unsigned tag, int lane) {
  bar();
  if constexpr (I == 1) {
    wait_vm<0>();
    if (yflag && lane == 0) st_dev(yflag, tag);
  }
  const unsigned w = wa + g2_wslot(I % 3), hh = ha + g2_hslot(I);
  g2_step<I, 0, 0>(Y, X, acc, w, hh, I > 0);
  g2_wait(X);
  g2_step<I, 1, 0>(X, Y, acc, w, hh, true);
  g2_wait(Y);
  if constexpr (I < 11) g2_iter<I + 1>(X, Y, acc, wa, ha, yflag, tag, lane);
}

__device__ __forceinline__ void g2_consumer(const Params& p, LAS char* lds, int row0, int cb8, int rt, int lane, unsigned tag, char* yflag) {
  const int g = lane >> 4, j = lane & 15;
  const int prow = row0 + rt * 16 + j;
  u32x2 qv[5];
#pragma unroll
  for (int ct = 0; ct < 5; ++ct) qv[ct] = *(const u32x2*)(p.q + (size_t)prow * D + cb8 * 80 + ct * 16 + 4 * g);
  const unsigned lb = (unsigned)(size_t)lds;
  f32x4 acc[5];
#pragma unroll
  for (int ct = 0; ct < 5; ++ct) acc[ct] = f32x4{0, 0, 0, 0};
  poll_flags(p.ws + WS_FLAGG + (row0 >> 5) * 4, 2, tag, lane, p.ws);
  const float gt = __uint_as_float(ld_dev(p.ws + WS_GATE + prow * 4));
  G2Frag X, Y;
  g2_iter<0>(X, Y, acc, lb + lane * 16, lb + rt * 4096 + lane * 16, yflag, tag, lane);
#pragma unroll
  for (int n = 0; n < 10; ++n) acc[n % 5] = MFMA(Y.w[n / 5][n % 5], Y.h[n / 5], acc[n % 5]);
  bar();
  const int qq = j >> 2, pp = lane & 3;
  const float omg = 1.f - gt;
#pragma unroll
  for (int ct = 0; ct < 5; ++ct) {
    const int yr = 16 * ct + 4 * g + qq, yc = 2 * rt + (pp >> 1);
    const v4i16 yv = __builtin_amdgcn_ds_read_tr16_b64_v4i16((LAS v4i16*)(lds + G2_YT + yr * 128 + ((yc ^ (((yr >> 1) & 3) << 1)) << 4) + 8 * (pp & 1)));
    const u32x2 y2 = __builtin_bit_cast(u32x2, yv);
    const f32x4 b = acc[ct];
    u32x2 w;
    w[0] = pk(gt * (blo(qv[ct][0]) * blo(y2[0])) + omg * b[0], gt * (bhi(qv[ct][0]) * bhi(y2[0])) + omg * b[1]);
    w[1] = pk(gt * (blo(qv[ct][1]) * blo(y2[1])) + omg * b[2], gt * (bhi(qv[ct][1]) * bhi(y2[1])) + omg * b[3]);
    *(u32x2*)(p.out + (size_t)prow * D + cb8 * 80 + ct * 16 + 4 * g) = w;
  }
}

__global__ __launch_bounds__(NTHR, 2) void gm_kernel(Params p) {
  extern __shared__ __attribute__((aligned(16))) char shm[];
  LAS char* lds = (LAS char*)shm;
  const int tid = threadIdx.x, wid = __builtin_amdgcn_readfirstlane(tid >> 6), lane = tid & 63;
  const int b = blockIdx.x, xcd = b & 7, r = b >> 3;
  STAMP(0);
  unsigned cpf = 0;
  if (wid == 1) {
    unsigned long long pc; asm volatile("s_getpc_b64 %0" : "=s"(pc));
    const char* a = (const char*)(pc & ~127ull) + (r * 11 + (lane < 11 ? lane : 10)) * 128;
    asm volatile("global_load_dword %0, %1, off" : "=v"(cpf) : "v"(a) : "memory");
  }
  unsigned epoch;
  asm volatile("s_load_dword %0, %1, 0x0" : "=s"(epoch) : "s"(p.ws) : "memory");

  const bool is_g1 = r < 24;
  const int rg = r & 3, cs = r >> 2;
  const bool conv = r < 20;
  const int ch0 = xcd * 80 + r * 4;
  const int row0 = (xcd >> 2) * 1024 + rg * 256, cb = 6 * (xcd & 3) + cs;
  if (is_g1 && wid >= 4) {
    const G1Off off = g1_offsets(lane, wid - 4);
    g1_issue<8>(p, lds, 0, row0, cb, wid - 4, off); g1_issue<8>(p, lds + G1_SLOT, 1, row0, cb, wid - 4, off);
    if (wid < 6) { const int unit = (xcd * 24 + r) * 2 + (wid - 4); repack_issue(p, lds + RP_IMG + (wid - 4) * 5120, unit >> 3, unit & 7, lane); }
  }
  asm volatile("s_waitcnt lgkmcnt(0)" : "+s"(epoch)::"memory");
  const unsigned tag = epoch + 1u;
  if (tid == 0) st_dev(p.ws + WS_ARRIVE + b * 4, tag);
  if (tid < 16) lds_signal(lds, tid, tag - 1u);
  STAMP(1);
  char* hflag = p.ws + WS_FLAGH + ((xcd >> 2) * 4 + rg) * 512 + (cb * 4 + wid) * 4;
  if (is_g1) {
    if (wid >= 4) {
      const int unit = (xcd * 24 + r) * 2 + (wid - 4);
      if (wid < 6) g1_loader<8, true>(p, lds, row0, cb, conv, ch0, wid - 4, unit, lane, tag);
      else g1_loader<8, false>(p, lds, row0, cb, conv, ch0, wid - 4, 0, lane, tag);
      STAMP(2);
    }
    else g1_consumer<8>(p, lds, row0, cb, wid, lane);
  } else {
    const int sidx = xcd * 8 + (r - 24);
    if (wid < 4) {
      PTile t;
      p_load(p, t, sidx * 5 + wid, lane);
      p_finish(p, t, sidx * 5 + wid, lane, tag);
    } else if (wid < 6) {
      PHalf t;
      const int tile = sidx * 5 + 4;
      ph_load(p, t, tile, wid - 4, lane);
      ph_finish(p, t, tile, wid - 4, lane);
      wait_vm<0>();
      if (wid == 5) lds_signal(lds, 8, tag);
      else { lds_await(lds, 8, tag); if (lane == 0) st_dev(p.ws + WS_FLAGP + (tile >> 5) * 128 + (tile & 31) * 4, tag); }
    } else {
      aux_gate(p, xcd * 256 + (r - 24) * 32 + (wid - 6) * 16, lane);
      wait_vm<0>();
      if (wid == 7) lds_signal(lds, 9, tag);
      else { lds_await(lds, 9, tag); if (lane == 0) st_dev(p.ws + WS_FLAGG + sidx * 4, tag); }
    }
    STAMP(2);
    if (b == 255 && wid == 1) {
      for (int i = 0; i < 4; ++i) poll_flags(p.ws + WS_ARRIVE + i * 256, 64, tag, lane, p.ws);
      if (lane == 0) st_dev(p.ws + WS_EPOCH, tag);
    }
  }
  STAMP(3);
  const int cb8 = r >> 2, orow0 = xcd * 256 + (r & 3) * 64;
  if (wid >= 4) {
    wait_vm<0>();
    bar();
    const int lw = wid - 4;
    if (lw == 0) { poll_flags6(p.ws + WS_FLAGW, tag, lane, p.ws); lds_signal(lds, 1, tag); }
    else lds_await(lds, 1, tag);
    STAMP(7);
    const rsrc_t rw = mkrs(p.ws, WS_END);
#pragma unroll
    for (int st = 0; st < 3; ++st) g2_issue_w(rw, WS_WDP + cb8 * 48 * 5120, lane * 16, lds, st, lw);
    const char* w = p.ws + WS_WDP + (size_t)cb8 * 48 * 5120 + 3 * 20480 + ((r & 3) * 360 + lw * 90) * 128;
    unsigned d0, d1;
    asm volatile("global_load_dword %0, %1, off sc1" : "=v"(d0) : "v"(w + lane * 128) : "memory");
    asm volatile("global_load_dword %0, %1, off sc1" : "=v"(d1) : "v"(w + (64 + (lane < 26 ? lane : 25)) * 128) : "memory");
    STAMP(8);
    if (lw == 0) {
      const char* f0 = p.ws + WS_FLAGH + (orow0 >> 8) * 512;
      poll_flags2(f0, 64, f0 + 256, 32, tag, lane, p.ws);
      lds_signal(lds, 2, tag);
    } else lds_await(lds, 2, tag);
    asm volatile("s_waitcnt vmcnt(0)" : "+v"(d0), "+v"(d1)::"memory");
    STAMP(9);
    const unsigned hsrc = WS_H + ((orow0 >> 4) + lw) * 48 * 1024;
    g2_issue_h(rw, hsrc, lane * 16, lds, 0, lw);
    if (conv) { lds_await(lds, 4, tag); lds_await(lds, 5, tag); lds_await(lds, 6, tag); lds_await(lds, 7, tag); }
    g2_issue_h(rw, hsrc, lane * 16, lds, 1, lw); g2_issue_h(rw, hsrc, lane * 16, lds, 2, lw);
  } else {
    bar();
    if (conv) conv_channel(p, lds, ch0 + wid, wid, lane, tag, hflag);
    else if (is_g1) { wait_vm<0>(); if (lane == 0) st_dev(hflag, tag); }
  }
  STAMP(4);
  bar();
  STAMP(5);
  if (wid < 4) g2_consumer(p, lds, orow0, cb8, wid, lane, tag, conv ? p.ws + WS_FLAGY + ((ch0 + wid) / 80) * 512 + ((ch0 + wid) % 80) * 4 : nullptr);
  else g2_loader(p, lds, orow0, cb8, wid - 4, lane, tag);
  STAMP(6);
  asm volatile("s_waitcnt vmcnt(0)" : "+v"(cpf)::"memory");
}
}

extern "C" void kernel_launch(void* const* d_in, const int* in_sizes, int n_in, void* d_out, int out_size, void* d_ws, size_t ws_size, hipStream_t stream) {
  using namespace gm;
  Params p;
  p.q = (const u16*)d_in[0]; p.k = (const u16*)d_in[1]; p.v = (const u16*)d_in[2]; p.sk = (const u16*)d_in[3];
  p.hk = (const u16*)d_in[4]; p.dv = (const u16*)d_in[5]; p.wg = (const u16*)d_in[7]; p.wu = (const u16*)d_in[8];
  p.wd = (const u16*)d_in[9]; p.gw = (const u16*)d_in[10]; p.out = (u16*)d_out; p.ws = (char*)d_ws;
  (void)hipFuncSetAttribute((const void*)gm_kernel, hipFuncAttributeMaxDynamicSharedMemorySize, LDS_BYTES);
  gm_kernel<<<NWG, NTHR, LDS_BYTES, stream>>>(p);
}
```

```cpp
#include <hip/hip_runtime.h>
#include <stdint.h>

#define PFTLB 1
#define PDELAY 0
namespace gm {
#define LAS __attribute__((address_space(3)))
typedef unsigned short u16;
typedef short bf16x8 __attribute__((ext_vector_type(8)));
typedef short v4i16 __attribute__((ext_vector_type(4)));
typedef float f32x4 __attribute__((ext_vector_type(4)));
typedef float f32x16 __attribute__((ext_vector_type(16)));
typedef float f32x2 __attribute__((ext_vector_type(2)));
typedef __bf16 bf16x2_t __attribute__((ext_vector_type(2)));
typedef unsigned u32x4 __attribute__((ext_vector_type(4)));
typedef unsigned u32x2 __attribute__((ext_vector_type(2)));

constexpr int S = 2048, D = 640, F = 1536;
constexpr int NWG = 256, NTHR = 512;
constexpr int LDS_BYTES = 163840;

constexpr unsigned WS_EPOCH = 0, WS_ARRIVE = 1024, WS_FLAGP = 4096, WS_FLAGH = 8192, WS_FLAGW = 20480, WS_FLAGG = 15360,
                   WS_FLAGY = 16384, WS_TMO = 24576, WS_GATE = 32768, WS_STAMP = 65536;
constexpr unsigned WS_KVR = 1u << 20, WS_YT = 4u << 20, WS_WDP = 8u << 20, WS_H = 12u << 20, WS_END = 20u << 20;

constexpr int G1_SLOT = 49152, G1_WG = 32768, G1_WU = 40960;
constexpr int RP_IMG = 147456;
constexpr int C_H = 98304, C_HSTR = 9216, C_H2 = 4640;
constexpr int C_K = 64512, C_KSTR = 8448, C_K1 = 4224;
constexpr int L_SIG = 163776;
constexpr int G2_YT = 84992;
__host__ __device__ constexpr int g2_hslot(int i) { return (i & 3) == 0 ? 40960 : ((i & 3) == 1 ? 135168 : ((i & 3) == 2 ? 98304 : 114688)); }
__host__ __device__ constexpr int g2_wslot(int i) { return i < 2 ? i * 20480 : 64512; }

struct Params {
  const u16 *q, *k, *v, *sk, *hk, *dv, *wg, *wu, *wd, *gw;
  u16* out;
  char* ws;
};

#define RLXA __ATOMIC_RELAXED, __HIP_MEMORY_SCOPE_AGENT
__device__ __forceinline__ unsigned ld_dev(const void* p) { return __hip_atomic_load((const unsigned*)p, RLXA); }
__device__ __forceinline__ void st_dev(void* p, unsigned v) { __hip_atomic_store((unsigned*)p, v, RLXA); }
__device__ __forceinline__ unsigned pk(float lo, float hi) { f32x2 v = {lo, hi}; return __builtin_bit_cast(unsigned, __builtin_convertvector(v, bf16x2_t)); }
__device__ __forceinline__ float blo(unsigned x) { return __uint_as_float(x << 16); }
__device__ __forceinline__ float bhi(unsigned x) { return __uint_as_float(x & 0xffff0000u); }
__device__ __forceinline__ float rb(float x) { return blo(pk(x, x)); }
template <int N> __device__ __forceinline__ void wait_vm() { asm volatile("s_waitcnt vmcnt(%0)" ::"n"(N) : "memory"); }
__device__ __forceinline__ void wait_lgkm0() { asm volatile("s_waitcnt lgkmcnt(0)" ::: "memory"); }
__device__ __forceinline__ void bar() { asm volatile("" ::: "memory"); __builtin_amdgcn_s_barrier(); asm volatile("" ::: "memory"); }
template <int AUX> __device__ __forceinline__ void dma16(const void* g, LAS char* l) {
  __builtin_amdgcn_global_load_lds((const unsigned*)g, (LAS unsigned*)l, 16, 0, AUX);
}
typedef __amdgpu_buffer_rsrc_t rsrc_t;
__device__ __forceinline__ rsrc_t mkrs(const void* base, unsigned bytes) { return __builtin_amdgcn_make_buffer_rsrc((void*)base, 0, bytes, 0x00020000); }
template <int AUX> __device__ __forceinline__ void bdma(rsrc_t rs, LAS char* l, unsigned voff, unsigned soff) {
  __builtin_amdgcn_raw_ptr_buffer_load_lds(rs, (LAS void*)l, 16, voff, soff, 0, AUX);
}
__device__ __forceinline__ bf16x8 lds16(const LAS char* p) { return *(const LAS bf16x8*)p; }
__device__ __forceinline__ bf16x8 ldstr(const LAS char* p0, const LAS char* p1) {
  v4i16 a = __builtin_amdgcn_ds_read_tr16_b64_v4i16((LAS v4i16*)p0), b = __builtin_amdgcn_ds_read_tr16_b64_v4i16((LAS v4i16*)p1);
  return __builtin_shufflevector(a, b, 0, 1, 2, 3, 4, 5, 6, 7);
}
template <int OFF> __device__ __forceinline__ void rd128(bf16x8& d, unsigned a) { asm volatile("ds_read_b128 %0, %1 offset:%2" : "=v"(d) : "v"(a), "n"(OFF)); }
template <int OFF> __device__ __forceinline__ void rd64(u32x2& d, unsigned a) { asm volatile("ds_read_b64 %0, %1 offset:%2" : "=v"(d) : "v"(a), "n"(OFF)); }
template <int OFF> __device__ __forceinline__ void rdtr(u32x2& d, unsigned a) { asm volatile("ds_read_b64_tr_b16 %0, %1 offset:%2" : "=v"(d) : "v"(a), "n"(OFF)); }
__device__ __forceinline__ bf16x8 cat(const u32x2& lo, const u32x2& hi) { return __builtin_bit_cast(bf16x8, u32x4{lo[0], lo[1], hi[0], hi[1]}); }
#define SCHED() __builtin_amdgcn_sched_barrier(0)
#define PIN(x) asm volatile("" : "+v"(x))
__device__ __forceinline__ void wait_vm0_seen() { __builtin_amdgcn_s_waitcnt(0x0F70); }
#define MFMA(a, b, c) __builtin_amdgcn_mfma_f32_16x16x32_bf16(a, b, c, 0, 0, 0)
#define MFMA32(a, b, c) __builtin_amdgcn_mfma_f32_32x32x16_bf16(a, b, c, 0, 0, 0)

#define STAMP(i) do {} while (0)
#define STAMP2(i) do {} while (0)

__device__ __forceinline__ void poll_flags(const char* flags, int n, unsigned tag, int lane, char* ws) {
#pragma unroll 1
  for (unsigned spins = 0;; ++spins) {
    unsigned f = tag;
    if (lane < n) f = ld_dev(flags + lane * 4);
    if (__all(f == tag)) break;
    if (spins > (1u << 16)) { if (lane == 0) st_dev(ws + WS_TMO, tag); break; }
    __builtin_amdgcn_s_sleep(1);
  }
}

__device__ __forceinline__ void lds_signal(LAS char* lds, int i, unsigned v) { *(volatile LAS unsigned*)(lds + L_SIG + 4 * i) = v; }
__device__ __forceinline__ void lds_await(LAS char* lds, int i, unsigned v) {
#pragma unroll 1
  for (unsigned n = 0; *(volatile LAS unsigned*)(lds + L_SIG + 4 * i) != v && n < (1u << 22); ++n) __builtin_amdgcn_s_sleep(1);
}
__device__ __forceinline__ void poll_flags2(const char* fa, int na, const char* fb, int nb, unsigned tag, int lane, char* ws) {
#pragma unroll 1
  for (unsigned spins = 0;; ++spins) {
    unsigned f = tag, g = tag;
    if (lane < na) f = ld_dev(fa + lane * 4);
    if (lane < nb) g = ld_dev(fb + lane * 4);
    if (__all(f == tag && g == tag)) break;
    if (spins > (1u << 16)) { if (lane == 0) st_dev(ws + WS_TMO, tag); break; }
    __builtin_amdgcn_s_sleep(1);
  }
}
__device__ __forceinline__ void poll_flags6(const char* f, unsigned tag, int lane, char* ws) {
  const unsigned long long t2 = ((unsigned long long)tag << 32) | tag;
#pragma unroll 1
  for (unsigned spins = 0;; ++spins) {
    const unsigned long long a = __hip_atomic_load((const unsigned long long*)(f + lane * 8), RLXA), b = __hip_atomic_load((const unsigned long long*)(f + 512 + lane * 8), RLXA),
                             c = __hip_atomic_load((const unsigned long long*)(f + 1024 + lane * 8), RLXA);
    if (__all(a == t2 && b == t2 && c == t2)) break;
    if (spins > (1u << 16)) { if (lane == 0) st_dev(ws + WS_TMO, tag); break; }
    __builtin_amdgcn_s_sleep(1);
  }
}
__device__ __forceinline__ unsigned flag_issue(const char* flags, int n, int lane) {
  unsigned f;
  const char* a = flags + (lane < n ? lane : 0) * 4;
  asm volatile("global_load_dword %0, %1, off sc1" : "=v"(f) : "v"(a) : "memory");
  return f;
}
template <int N> __device__ __forceinline__ void wait_vm_for(unsigned& f) { asm volatile("s_waitcnt vmcnt(%1)" : "+v"(f) : "n"(N) : "memory"); }
template <int N> __device__ __forceinline__ void wait_vm_for(unsigned& f, unsigned& g) { asm volatile("s_waitcnt vmcnt(%2)" : "+v"(f), "+v"(g) : "n"(N) : "memory"); }

__device__ __forceinline__ f32x2 up2(unsigned x) { return f32x2{blo(x), bhi(x)}; }
struct PTile { u32x4 kr[11], vr[8], wr[4]; };
__device__ __forceinline__ void p_load(const Params& p, PTile& t, int tile, int lane) {
  const int cbk = tile >> 5, pbk = tile & 31;
  const int pb = lane & 7, cc = lane >> 3;
  const int d0 = cbk * 64 + cc * 8, sb = pbk * 64 + pb * 8;
#pragma unroll
  for (int i = 0; i < 4; ++i) t.wr[i] = *(const u32x4*)(p.sk + d0 * 4 + i * 8);
#pragma unroll
  for (int i = 0; i < 11; ++i) { int s = sb - 3 + i; s = s < 0 ? 0 : s; t.kr[i] = *(const u32x4*)(p.k + (size_t)s * D + d0); }
#pragma unroll
  for (int i = 0; i < 8; ++i) t.vr[i] = *(const u32x4*)(p.v + (size_t)(sb + i) * D + d0);
}
struct PHalf { u32x2 kr[11], vr[8]; u32x4 wr[2]; };
__device__ __forceinline__ void ph_load(const Params& p, PHalf& t, int tile, int hf, int lane) {
  const int cbk = tile >> 5, pbk = tile & 31;
  const int d0 = cbk * 64 + (lane >> 3) * 8 + 4 * hf, sb = pbk * 64 + (lane & 7) * 8;
  t.wr[0] = *(const u32x4*)(p.sk + d0 * 4); t.wr[1] = *(const u32x4*)(p.sk + d0 * 4 + 8);
#pragma unroll
  for (int i = 0; i < 11; ++i) { int s = sb - 3 + i; s = s < 0 ? 0 : s; t.kr[i] = *(const u32x2*)(p.k + (size_t)s * D + d0); }
#pragma unroll
  for (int i = 0; i < 8; ++i) t.vr[i] = *(const u32x2*)(p.v + (size_t)(sb + i) * D + d0);
}
__device__ __forceinline__ void ph_finish(const Params& p, PHalf& t, int tile, int hf, int lane) {
  const int cbk = tile >> 5, pbk = tile & 31;
  const int d0 = cbk * 64 + (lane >> 3) * 8 + 4 * hf, sb = pbk * 64 + (lane & 7) * 8;
  if (sb == 0) { t.kr[0] = u32x2{0, 0}; t.kr[1] = t.kr[0]; t.kr[2] = t.kr[0]; }
  auto rs = __builtin_amdgcn_make_buffer_rsrc(p.ws, 0, WS_END, 0x00020000);
#pragma unroll
  for (int c = 0; c < 2; ++c) {
    const f32x2 w0 = {blo(t.wr[c][0]), blo(t.wr[c][2])}, w1 = {bhi(t.wr[c][0]), bhi(t.wr[c][2])};
    const f32x2 w2 = {blo(t.wr[c][1]), blo(t.wr[c][3])}, w3 = {bhi(t.wr[c][1]), bhi(t.wr[c][3])};
    f32x2 kf[11], o[8];
#pragma unroll
    for (int i = 0; i < 11; ++i) kf[i] = up2(t.kr[i][c]);
#pragma unroll
    for (int i = 0; i < 8; ++i) o[i] = (w3 * kf[i] + w2 * kf[i + 1] + w1 * kf[i + 2] + w0 * kf[i + 3]) * up2(t.vr[i][c]);
    const u32x4 lo = {pk(o[7].x, o[6].x), pk(o[5].x, o[4].x), pk(o[3].x, o[2].x), pk(o[1].x, o[0].x)};
    const u32x4 hi = {pk(o[7].y, o[6].y), pk(o[5].y, o[4].y), pk(o[3].y, o[2].y), pk(o[1].y, o[0].y)};
    __builtin_amdgcn_raw_buffer_store_b128(lo, rs, WS_KVR + ((d0 + 2 * c) * S + (S - 8 - sb)) * 2, 0, 16);
    __builtin_amdgcn_raw_buffer_store_b128(hi, rs, WS_KVR + ((d0 + 2 * c + 1) * S + (S - 8 - sb)) * 2, 0, 16);
  }
}
__device__ __forceinline__ void p_finish(const Params& p, PTile& t, int tile, int lane, unsigned tag) {
  const int cbk = tile >> 5, pbk = tile & 31;
  const int pb = lane & 7, cc = lane >> 3;
  const int d0 = cbk * 64 + cc * 8, sb = pbk * 64 + pb * 8;
  if (sb == 0) { t.kr[0] = u32x4{0, 0, 0, 0}; t.kr[1] = t.kr[0]; t.kr[2] = t.kr[0]; }
  auto rs = __builtin_amdgcn_make_buffer_rsrc(p.ws, 0, WS_END, 0x00020000);
#pragma unroll
  for (int c = 0; c < 4; ++c) {
    const f32x2 w0 = {blo(t.wr[c][0]), blo(t.wr[c][2])}, w1 = {bhi(t.wr[c][0]), bhi(t.wr[c][2])};
    const f32x2 w2 = {blo(t.wr[c][1]), blo(t.wr[c][3])}, w3 = {bhi(t.wr[c][1]), bhi(t.wr[c][3])};
    f32x2 kf[11], o[8];
#pragma unroll
    for (int i = 0; i < 11; ++i) kf[i] = up2(t.kr[i][c]);
#pragma unroll
    for (int i = 0; i < 8; ++i) o[i] = (w3 * kf[i] + w2 * kf[i + 1] + w1 * kf[i + 2] + w0 * kf[i + 3]) * up2(t.vr[i][c]);
    const u32x4 lo = {pk(o[7].x, o[6].x), pk(o[5].x, o[4].x), pk(o[3].x, o[2].x), pk(o[1].x, o[0].x)};
    const u32x4 hi = {pk(o[7].y, o[6].y), pk(o[5].y, o[4].y), pk(o[3].y, o[2].y), pk(o[1].y, o[0].y)};
    __builtin_amdgcn_raw_buffer_store_b128(lo, rs, WS_KVR + ((d0 + 2 * c) * S + (S - 8 - sb)) * 2, 0, 16);
    __builtin_amdgcn_raw_buffer_store_b128(hi, rs, WS_KVR + ((d0 + 2 * c + 1) * S + (S - 8 - sb)) * 2, 0, 16);
  }
  STAMP(11);
  wait_vm<0>();
  if (lane == 0) st_dev(p.ws + WS_FLAGP + cbk * 128 + pbk * 4, tag);
}

__device__ __forceinline__ void repack_issue(const Params& p, LAS char* img, int ks, int cb8, int lane) {
  const rsrc_t rd = mkrs(p.wd, F * D * 2);
#pragma unroll
  for (int j = 0; j < 5; ++j) {
    const int n = 64 * j + lane, row = n / 10, c = n - row * 10;
    bdma<0>(rd, img + j * 1024, row * (D * 2) + c * 16, ks * 32 * (D * 2) + cb8 * 160);
  }
}
struct RpAddr { unsigned a0; unsigned o[5]; };
__device__ __forceinline__ RpAddr repack_addr(LAS char* img, int ks, int cb8, int lane) {
  const int g = lane >> 4, qq = (lane & 15) >> 2, pp = lane & 3;
  RpAddr r;
  r.a0 = (unsigned)(size_t)img + (16 * (g >> 1) + 4 * (g & 1) + qq) * 160 + 8 * pp;
#pragma unroll
  for (int ct = 0; ct < 5; ++ct) r.o[ct] = WS_WDP + (((cb8 * 48 + ks) * 5 + ct) * 64 + lane) * 16;
  return r;
}
__device__ __forceinline__ void repack_store(const Params& p, const RpAddr& r) {
  u32x2 lo[5], hi[5];
  rdtr<0>(lo[0], r.a0); rdtr<1280>(hi[0], r.a0); rdtr<32>(lo[1], r.a0); rdtr<1312>(hi[1], r.a0); rdtr<64>(lo[2], r.a0); rdtr<1344>(hi[2], r.a0);
  rdtr<96>(lo[3], r.a0); rdtr<1376>(hi[3], r.a0); rdtr<128>(lo[4], r.a0); rdtr<1408>(hi[4], r.a0);
  asm volatile("s_waitcnt lgkmcnt(0)" : "+v"(lo[0]), "+v"(lo[1]), "+v"(lo[2]), "+v"(lo[3]), "+v"(lo[4]), "+v"(hi[0]), "+v"(hi[1]), "+v"(hi[2]), "+v"(hi[3]), "+v"(hi[4])::"memory");
  auto rs = __builtin_amdgcn_make_buffer_rsrc(p.ws, 0, WS_END, 0x00020000);
#pragma unroll
  for (int ct = 0; ct < 5; ++ct) __builtin_amdgcn_raw_buffer_store_b128(u32x4{lo[ct][0], lo[ct][1], hi[ct][0], hi[ct][1]}, rs, r.o[ct], 0, 16);
}
__device__ __forceinline__ void repack_finish(const Params& p, LAS char* img, int ks, int cb8, int lane) {
  wait_vm0_seen();
  auto rs = __builtin_amdgcn_make_buffer_rsrc(p.ws, 0, WS_END, 0x00020000);
  const int g = lane >> 4, qq = (lane & 15) >> 2, pp = lane & 3;
  const int r0 = 16 * (g >> 1) + 4 * (g & 1) + qq;
  const LAS char* a0 = img + r0 * 160 + 8 * pp;
#pragma unroll
  for (int ct = 0; ct < 5; ++ct) {
    const bf16x8 f = ldstr(a0 + ct * 32, a0 + 8 * 160 + ct * 32);
    __builtin_amdgcn_raw_buffer_store_b128(__builtin_bit_cast(u32x4, f), rs, WS_WDP + (((cb8 * 48 + ks) * 5 + ct) * 64 + lane) * 16, 0, 16);
  }
}

__device__ __forceinline__ void aux_gate(const Params& p, int row0, int lane) {
  const int r4 = lane >> 4, c16 = lane & 15;
  u32x4 gw[5], x[4][5];
#pragma unroll
  for (int i = 0; i < 5; ++i) {
    gw[i] = *(const u32x4*)(p.gw + (c16 + 16 * i) * 8);
#pragma unroll
    for (int it = 0; it < 4; ++it) x[it][i] = *(const u32x4*)(p.q + (size_t)(row0 + 4 * it + r4) * D + (c16 + 16 * i) * 8);
  }
#pragma unroll
  for (int it = 0; it < 4; ++it) {
    float z = 0.f;
#pragma unroll
    for (int i = 0; i < 5; ++i)
#pragma unroll
      for (int j = 0; j < 4; ++j) z += blo(x[it][i][j]) * blo(gw[i][j]) + bhi(x[it][i][j]) * bhi(gw[i][j]);
    z += __shfl_xor(z, 1); z += __shfl_xor(z, 2); z += __shfl_xor(z, 4); z += __shfl_xor(z, 8);
    z = rb(z);
    const float gt = rb(1.f / (1.f + __expf(-z)));
    if (c16 == 0) st_dev(p.ws + WS_GATE + (row0 + it * 4 + r4) * 4, __float_as_uint(gt));
  }
}

struct G1Off { unsigned x, g, u; };
__device__ __forceinline__ G1Off g1_offsets(int lane, int lw) {
  const int rl = lane >> 3, c = lane & 7;
  return G1Off{(unsigned)(rl * (D * 2) + ((c ^ ((4 * (lw & 1) + (rl >> 1)) & 7)) << 4)),
               (unsigned)(rl * (F * 2) + ((c ^ (((rl >> 1) & 1) << 1)) << 4)),
               (unsigned)(rl * (F * 2) + ((c ^ ((((rl >> 1) & 1) | 2) << 1)) << 4))};
}
template <int NX>
__device__ __forceinline__ void g1_issue(const Params& p, LAS char* slot, int ks, int row0, int cb, int lw, const G1Off& o) {
  const rsrc_t rq = mkrs(p.q, S * D * 2), rg = mkrs(p.wg, D * F * 2), ru = mkrs(p.wu, D * F * 2);
  const unsigned xb = row0 * (D * 2) + ks * 128 + lw * (8 * D * 2);
  const unsigned wo = (64 * ks + 16 * lw) * (F * 2) + cb * 128;
  bdma<0>(rg, slot + G1_WG + (2 * lw) * 1024, o.g, wo);
  bdma<0>(ru, slot + G1_WU + (2 * lw) * 1024, o.u, wo);
  bdma<0>(rg, slot + G1_WG + (2 * lw + 1) * 1024, o.g, wo + 8 * (F * 2));
  bdma<0>(ru, slot + G1_WU + (2 * lw + 1) * 1024, o.u, wo + 8 * (F * 2));
#pragma unroll
  for (int i = 0; i < NX; ++i) bdma<0>(rq, slot + (lw + 4 * i) * 1024, o.x, xb + i * (32 * D * 2));
}

__device__ __forceinline__ void ldsw128(unsigned a, u32x4 v) { asm volatile("ds_write_b128 %0, %1" ::"v"(a), "v"(v) : "memory"); }
__device__ __forceinline__ void conv_issue_k(const Params& p, LAS char* lds, int ch, int lw, int lane) {
  const rsrc_t rw = mkrs(p.ws, WS_END);
  const unsigned so = WS_KVR + ch * (S * 2), vo = lane * 16;
  LAS char* d = lds + C_K + lw * C_KSTR;
#pragma unroll
  for (int j = 0; j < 4; ++j) bdma<16>(rw, d + 16 + C_K1 + j * 1024, vo, so + j * 1024);
#pragma unroll
  for (int j = 0; j < 4; ++j) bdma<16>(rw, d + 16 + j * 1024, vo, so + j * 1024 + 2);
  if (lane < 8) ldsw128((unsigned)(size_t)d + (lane >> 2) * C_K1 + (257 + (lane & 3)) * 16, u32x4{0, 0, 0, 0});
}
template <int NX>
__device__ __forceinline__ void g1_loader(const Params& p, LAS char* lds, int row0, int cb, bool conv, int ch0, int lw, int lane, unsigned tag) {
  constexpr int NP = NX + 4;
  const G1Off off = g1_offsets(lane, lw);
  const int ch = min(ch0 + lw, D - 1);
  const char* touch = (const char*)p.hk + (size_t)ch * (S * 2) + (lane & 31) * 128;
  const char* fpa = p.ws + WS_FLAGP + (min(ch0, D - 4) >> 6) * 128 + (lane & 31) * 4;
  unsigned dummy, fp = tag;
  wait_vm<NP>(); bar(); g1_issue<NX>(p, lds + 2 * G1_SLOT, 2, row0, cb, lw, off);
  wait_vm<NP>(); bar(); g1_issue<NX>(p, lds, 3, row0, cb, lw, off);
  wait_vm<NP>(); bar(); g1_issue<NX>(p, lds + G1_SLOT, 4, row0, cb, lw, off);
  wait_vm<NP>(); bar(); g1_issue<NX>(p, lds + 2 * G1_SLOT, 5, row0, cb, lw, off);
  wait_vm<NP>(); bar(); g1_issue<NX>(p, lds, 6, row0, cb, lw, off);
  wait_vm<NP>(); bar(); g1_issue<NX>(p, lds + G1_SLOT, 7, row0, cb, lw, off);
  asm volatile("global_load_dword %0, %1, off" : "=v"(dummy) : "v"(touch) : "memory");
  wait_vm<NP + 1>(); bar(); g1_issue<NX>(p, lds + 2 * G1_SLOT, 8, row0, cb, lw, off);
  wait_vm<NP + 1>(); bar();
  if (conv && lw == 0) asm volatile("global_load_dword %0, %1, off sc1" : "=v"(fp) : "v"(fpa) : "memory");
  g1_issue<NX>(p, lds, 9, row0, cb, lw, off);
  asm volatile("s_waitcnt vmcnt(%2)" : "+v"(fp), "+v"(dummy) : "n"(NP) : "memory");
  if (conv && lw == 0 && __all(fp == tag)) lds_signal(lds, 0, tag);
  wait_lgkm0();
  bar();
  const bool early = conv && *(volatile LAS unsigned*)(lds + L_SIG) == tag;
  if (early) { conv_issue_k(p, lds, ch, lw, lane); wait_vm<8>(); } else wait_vm<0>();
  wait_lgkm0();
  bar();
  if (conv) {
    const rsrc_t rh = mkrs(p.hk, D * S * 2);
    const unsigned so = ch * (S * 2), vo = lane * 16, vz = lane ? lane * 16 - 4 : 0;
    LAS char* d = lds + C_H + lw * C_HSTR;
    bdma<0>(rh, d + 448, vz, so);
#pragma unroll
    for (int j = 1; j < 4; ++j) bdma<0>(rh, d + 448 + j * 1024, vo, so + j * 1024 - 4);
#pragma unroll
    for (int j = 0; j < 4; ++j) bdma<0>(rh, d + 448 + C_H2 + j * 1024, vo, so + j * 1024);
    if (lane < 56) ldsw128((unsigned)(size_t)d + (lane >= 28 ? C_H2 + (lane - 28) * 16 : lane * 16), u32x4{0, 0, 0, 0});
    if (!early) {
      if (lw == 0) { poll_flags(p.ws + WS_FLAGP + (ch0 >> 6) * 128, 32, tag, lane, p.ws); lds_signal(lds, 3, tag); }
      else lds_await(lds, 3, tag);
      conv_issue_k(p, lds, ch, lw, lane);
    }
    wait_lgkm0();
  }
}

template <int NRT> struct G1Frag { u32x2 wl, wh; bf16x8 x[NRT]; };
template <int NRT, int KK, int I>
__device__ __forceinline__ void g1_read1(G1Frag<NRT>& f, unsigned wa, unsigned xa) {
  if constexpr (I == 0) rdtr<KK * 2048>(f.wl, wa);
  else if constexpr (I == 1) rdtr<KK * 2048 + 512>(f.wh, wa);
  else rd128<(I - 2) * 4096>(f.x[I - 2], xa);
}
template <int NRT, int KK, int N, bool LAST>
__device__ __forceinline__ void g1_step(G1Frag<NRT>& c, G1Frag<NRT>& nx, f32x16 (&acc)[NRT], unsigned wa, unsigned xa) {
  constexpr int NR = NRT + 2, PER = LAST ? 3 : 2;
  static_assert(NRT == 8, "");
  if constexpr (N == 0) asm volatile("s_waitcnt lgkmcnt(4)" : "+v"(c.wl), "+v"(c.wh), "+v"(c.x[0]), "+v"(c.x[1]), "+v"(c.x[2]), "+v"(c.x[3]));
  if constexpr (N == 4) asm volatile("s_waitcnt lgkmcnt(%4)" : "+v"(c.x[4]), "+v"(c.x[5]), "+v"(c.x[6]), "+v"(c.x[7]) : "n"(4 * PER < NR ? 4 * PER : NR));
  if constexpr (PER * N < NR) g1_read1<NRT, KK, PER * N>(nx, wa, xa);
  if constexpr (PER * N + 1 < NR) g1_read1<NRT, KK, PER * N + 1>(nx, wa, xa);
  if constexpr (PER == 3 && PER * N + 2 < NR) g1_read1<NRT, KK, PER * N + 2>(nx, wa, xa);
  acc[N] = MFMA32(cat(c.wl, c.wh), c.x[N], acc[N]);
  SCHED();
  if constexpr (N + 1 < NRT) g1_step<NRT, KK, N + 1, LAST>(c, nx, acc, wa, xa);
  else {
    asm volatile("" ::"v"(c.wl), "v"(c.wh), "v"(c.x[0]), "v"(c.x[1]), "v"(c.x[2]), "v"(c.x[3]));
    asm volatile("" ::"v"(c.x[4]), "v"(c.x[5]), "v"(c.x[6]), "v"(c.x[7]));
  }
}

template <int NRT>
__device__ __forceinline__ void g1_consumer(const Params& p, LAS char* lds, int row0, int cb, int w, int lane) {
  const int j = lane & 31, h = lane >> 5, mat = (lane >> 4) & 1, qq = (lane & 15) >> 2, pp = lane & 3;
  const unsigned lb = (unsigned)(size_t)lds;
  const unsigned xl = j * 128 + ((h ^ ((j >> 1) & 7)) << 4);
  const unsigned wa0 = lb + G1_WG + mat * 8192 + (8 * h + qq) * 128 + 32 * (w ^ (((qq >> 1) & 1) | (mat << 1))) + 8 * pp;
  f32x16 acc[NRT];
  G1Frag<NRT> A, B;
#pragma unroll
  for (int m = 0; m < NRT; ++m) {
#pragma unroll
    for (int e = 0; e < 16; ++e) acc[m][e] = 0.f;
    B.x[m] = bf16x8{0, 0, 0, 0, 0, 0, 0, 0};
  }
  B.wl = u32x2{0, 0}; B.wh = u32x2{0, 0};
  int slot = 0;
#pragma unroll 1
  for (int s = 0; s < 10; ++s) {
    bar();
    STAMP2(s);
    const unsigned wa = wa0 + slot, xb = lb + slot;
    g1_step<NRT, 0, 0, false>(B, A, acc, wa, xb + xl);
    g1_step<NRT, 1, 0, false>(A, B, acc, wa, xb + (xl ^ 32));
    g1_step<NRT, 2, 0, false>(B, A, acc, wa, xb + (xl ^ 64));
    g1_step<NRT, 3, 0, true>(A, B, acc, wa, xb + (xl ^ 96));
    asm volatile("s_waitcnt lgkmcnt(0)" : "+v"(B.wl), "+v"(B.wh), "+v"(B.x[0]), "+v"(B.x[1]), "+v"(B.x[2]), "+v"(B.x[3])::"memory");
    asm volatile("" : "+v"(B.x[4]), "+v"(B.x[5]), "+v"(B.x[6]), "+v"(B.x[7]));
    slot = slot == 2 * G1_SLOT ? 0 : slot + G1_SLOT;
  }
#pragma unroll
  for (int m = 0; m < NRT; ++m) acc[m] = MFMA32(cat(B.wl, B.wh), B.x[m], acc[m]);
  auto rs = __builtin_amdgcn_make_buffer_rsrc(p.ws, 0, WS_END, 0x00020000);
#pragma unroll
  for (int m = 0; m < NRT; ++m) {
    u32x4 o;
#pragma unroll
    for (int e = 0; e < 4; ++e) o[e] = pk(acc[m][2 * e] * acc[m][8 + 2 * e], acc[m][2 * e + 1] * acc[m][8 + 2 * e + 1]);
    const int rtile = (row0 >> 4) + 2 * m + (j >> 4);
    __builtin_amdgcn_raw_buffer_store_b128(o, rs, WS_H + ((rtile * 48 + 2 * cb + (w >> 1)) * 64 + (2 * (w & 1) + h) * 16 + (j & 15)) * 16, 0, 16);
  }
  STAMP(2);
}

struct CSet { bf16x8 a[8]; u32x2 bl[8], bh[8]; };
template <int R, int K> __device__ __forceinline__ void conv_read1(CSet& s, unsigned A0, unsigned B0) {
  if constexpr (K < 8) rd128<64 * R + 512 * K>(s.a[K], A0);
  else if constexpr (((K - 8) & 1) == 0) rd64<64 * R + 512 * (7 - (K - 8) / 2)>(s.bl[(K - 8) / 2], B0);
  else rd64<64 * R + 512 * (7 - (K - 8) / 2) + 8>(s.bh[(K - 8) / 2], B0);
}
template <int R, int K> __device__ __forceinline__ void conv_reads(CSet& s, unsigned A0, unsigned B0) {
  conv_read1<R, K>(s, A0, B0);
  if constexpr (K < 23) conv_reads<R, K + 1>(s, A0, B0);
}
__device__ __forceinline__ void conv_wait(CSet& s) {
  asm volatile("s_waitcnt lgkmcnt(0)" : "+v"(s.a[0]), "+v"(s.a[1]), "+v"(s.a[2]), "+v"(s.a[3]), "+v"(s.a[4]), "+v"(s.a[5]), "+v"(s.a[6]), "+v"(s.a[7]),
               "+v"(s.bl[0]), "+v"(s.bl[1]), "+v"(s.bl[2]), "+v"(s.bl[3]), "+v"(s.bl[4]), "+v"(s.bl[5]), "+v"(s.bl[6]), "+v"(s.bl[7])::"memory");
  asm volatile("" : "+v"(s.bh[0]), "+v"(s.bh[1]), "+v"(s.bh[2]), "+v"(s.bh[3]), "+v"(s.bh[4]), "+v"(s.bh[5]), "+v"(s.bh[6]), "+v"(s.bh[7]));
}
constexpr int conv_pa(int n) { int a = 0; while (n >= 8 - a) { n -= 8 - a; ++a; } return a; }
constexpr int conv_pe(int n) { int a = 0; while (n >= 8 - a) { n -= 8 - a; ++a; } return n; }
template <int R, int N> __device__ __forceinline__ void conv_step(const CSet& c, CSet& nx, f32x4 (&acc)[8], unsigned A0, unsigned B0) {
  if constexpr (R < 7 && N < 24) conv_read1<R + 1, N>(nx, A0, B0);
  if constexpr (R == 7 && N < 16) {
    if constexpr (N & 1) rd64<512 * (7 - N / 2)>(nx.bh[N / 2], B0); else rd64<512 * (7 - N / 2) + 64>(nx.bl[N / 2], B0);
  }
  constexpr int a = conv_pa(N), e = conv_pe(N);
  acc[a + e] = MFMA(c.a[a], cat(c.bl[e], c.bh[e]), acc[a + e]);
  SCHED();
  if constexpr (N < 35) conv_step<R, N + 1>(c, nx, acc, A0, B0);
  else {
    asm volatile("" ::"v"(c.a[0]), "v"(c.a[1]), "v"(c.a[2]), "v"(c.a[3]), "v"(c.a[4]), "v"(c.a[5]), "v"(c.a[6]), "v"(c.a[7]),
                 "v"(c.bl[0]), "v"(c.bl[1]), "v"(c.bl[2]), "v"(c.bl[3]), "v"(c.bl[4]), "v"(c.bl[5]), "v"(c.bl[6]), "v"(c.bl[7]));
    asm volatile("" ::"v"(c.bh[0]), "v"(c.bh[1]), "v"(c.bh[2]), "v"(c.bh[3]), "v"(c.bh[4]), "v"(c.bh[5]), "v"(c.bh[6]), "v"(c.bh[7]));
  }
}
template <int R> __device__ __forceinline__ void conv_res(const CSet& c, CSet& nx, f32x4 (&acc)[8], unsigned A0, unsigned B0) {
  conv_step<R, 0>(c, nx, acc, A0, B0);
  conv_wait(nx);
}

__device__ __forceinline__ void conv_channel(const Params& p, LAS char* lds, int ch, int c, int lane, unsigned tag) {
  LAS char* H = lds + C_H + c * C_HSTR;
  LAS char* K = lds + C_K + c * C_KSTR;
  {
    if (lane == 0) {
      const u32x4 t = *(LAS u32x4*)(H + C_H2 + 448);
      *(LAS u32x4*)(H + 448) = u32x4{0u, t[0], t[1], t[2]};
      *(LAS u16*)(K + 14) = *(LAS u16*)(K + C_K1 + 16);
      *(LAS u16*)(K + 2055 * 2) = 0;
    }
    wait_lgkm0();
    __builtin_amdgcn_sched_barrier(0);
  }
  STAMP(7);
  const unsigned dw = ((const __attribute__((address_space(4))) unsigned*)(size_t)p.dv)[ch >> 1];
  const float dd = (ch & 1) ? bhi(dw) : blo(dw);
  const int rc = lane & 15, g = lane >> 4;
  const unsigned A0 = (unsigned)(size_t)H + (rc & 1) * C_H2 + 64 * (rc >> 1) + 16 * g;
  const unsigned B0 = (unsigned)(size_t)K + (rc & 1) * C_K1 + 72 - 8 * (rc >> 1) + 16 * g;
  f32x4 acc[8];
#pragma unroll
  for (int t = 0; t < 8; ++t) acc[t] = f32x4{0, 0, 0, 0};
  CSet X, Y2;
  conv_reads<0, 0>(X, A0, B0);
  conv_wait(X);
  conv_res<0>(X, Y2, acc, A0, B0);
  conv_res<1>(Y2, X, acc, A0, B0);
  conv_res<2>(X, Y2, acc, A0, B0);
  conv_res<3>(Y2, X, acc, A0, B0);
  conv_res<4>(X, Y2, acc, A0, B0);
  conv_res<5>(Y2, X, acc, A0, B0);
  conv_res<6>(X, Y2, acc, A0, B0);
  const unsigned KB = (unsigned)(size_t)K + (rc & 1) * C_K1 + 2 * (2052 - 64 * g - 4 * (rc >> 1) - 32 - 1792);
  conv_res<7>(Y2, X, acc, A0, KB);
  lds_signal(lds, 4 + c, tag);
  STAMP(8);
  LAS char* Y = K;
  const bool odd = rc & 1;
#pragma unroll
  for (int t = 0; t < 8; ++t) {
    const float v0 = acc[t][0] + blo(X.bl[t][1]) * dd, v1 = acc[t][1] + blo(X.bl[t][0]) * dd;
    const float v2 = acc[t][2] + blo(X.bh[t][1]) * dd, v3 = acc[t][3] + blo(X.bh[t][0]) * dd;
    const unsigned x01 = pk(v0, v1), x23 = pk(v2, v3);
    const unsigned rcv = (unsigned)__builtin_amdgcn_mov_dpp((int)(odd ? x01 : x23), 0xB1, 0xF, 0xF, true);
    const unsigned a = odd ? rcv : x01, bb = odd ? x23 : rcv;
    *(LAS u32x2*)(Y + 2 * (256 * t + 64 * g + 32 * (rc & 1) + 4 * (rc >> 1))) = u32x2{__builtin_amdgcn_perm(bb, a, 0x05040100u), __builtin_amdgcn_perm(bb, a, 0x07060302u)};
  }
  wait_lgkm0();
  __builtin_amdgcn_sched_barrier(0);
  auto rs = __builtin_amdgcn_make_buffer_rsrc(p.ws, 0, WS_END, 0x00020000);
#pragma unroll
  for (int it = 0; it < 4; ++it) {
    const int pos = 8 * (lane + 64 * it);
    __builtin_amdgcn_raw_buffer_store_b128(*(const LAS u32x4*)(Y + 2 * pos), rs, WS_YT + (ch * S + pos) * 2, 0, 16);
  }
  STAMP(9);
}

__device__ __forceinline__ void g2_issue_w(rsrc_t rw, unsigned wsrc, unsigned vo, LAS char* lds, int i, int lw) {
  LAS char* d = lds + g2_wslot(i % 3);
#pragma unroll
  for (int j = 0; j < 5; ++j) bdma<16>(rw, d + (lw * 5 + j) * 1024, vo, wsrc + i * 20480 + (lw * 5 + j) * 1024);
}
__device__ __forceinline__ void g2_issue_h(rsrc_t rw, unsigned hsrc, unsigned vo, LAS char* lds, int i, int lw) {
#pragma unroll
  for (int j = 0; j < 4; ++j) bdma<16>(rw, lds + g2_hslot(i) + lw * 4096 + j * 1024, vo, hsrc + i * 4096 + j * 1024);
}
__device__ __forceinline__ void g2_loader(const Params& p, LAS char* lds, int row0, int cb8, int lw, int lane, unsigned tag) {
  const rsrc_t rw = mkrs(p.ws, WS_END);
  const unsigned hsrc = WS_H + ((row0 >> 4) + lw) * 48 * 1024, wsrc = WS_WDP + cb8 * 48 * 5120, vo = lane * 16;
  unsigned fy0 = tag, fy1 = tag;
  g2_issue_w(rw, wsrc, vo, lds, 2, lw);
  wait_vm<17>(); bar();
  wait_vm<13>(); bar();
  g2_issue_w(rw, wsrc, vo, lds, 3, lw); g2_issue_h(rw, hsrc, vo, lds, 4, lw);
  STAMP(10);
  wait_vm<9>(); bar();
  g2_issue_w(rw, wsrc, vo, lds, 4, lw); g2_issue_h(rw, hsrc, vo, lds, 5, lw);
#pragma unroll 1
  for (int i = 3; i <= 6; ++i) {
    wait_vm<13>(); bar();
    g2_issue_w(rw, wsrc, vo, lds, i + 2, lw); g2_issue_h(rw, hsrc, vo, lds, i + 3, lw);
    if (i == 5) { fy0 = flag_issue(p.ws + WS_FLAGY + cb8 * 512, 64, lane); fy1 = flag_issue(p.ws + WS_FLAGY + cb8 * 512 + 256, 16, lane); }
  }
  STAMP(11);
  wait_vm_for<9>(fy0, fy1);
  if (!__all(fy0 == tag && fy1 == tag)) { poll_flags(p.ws + WS_FLAGY + cb8 * 512, 64, tag, lane, p.ws); poll_flags(p.ws + WS_FLAGY + cb8 * 512 + 256, 16, tag, lane, p.ws); }
  bar();
  g2_issue_w(rw, wsrc, vo, lds, 9, lw); g2_issue_h(rw, hsrc, vo, lds, 10, lw);
  {
    const int rl = lane >> 3, c = (lane & 7) ^ (((rl >> 1) & 3) << 1);
    const unsigned vy = rl * (S * 2) + c * 16, sy = WS_YT + cb8 * 80 * (S * 2) + row0 * 2;
#pragma unroll
    for (int j = 0; j < 3; ++j) { const int pc = min(lw * 3 + j, 9); bdma<16>(rw, lds + G2_YT + pc * 1024, vy, sy + pc * 8 * (S * 2)); }
  }
  wait_vm<16>(); bar();
  g2_issue_w(rw, wsrc, vo, lds, 10, lw); g2_issue_h(rw, hsrc, vo, lds, 11, lw);
  wait_vm<16>(); bar();
  g2_issue_w(rw, wsrc, vo, lds, 11, lw);
  STAMP(12);
  wait_vm<9>(); bar();
  wait_vm<0>(); bar();
}

struct G2Frag { bf16x8 w[2][5], h[2]; };
template <int I, int HALF, int N> __device__ __forceinline__ void g2_read1(G2Frag& f, unsigned wa, unsigned ha) {
  constexpr int H = HALF * 2048, k = N / 6, j = N % 6;
  if constexpr (j == 0) rd128<H + k * 1024>(f.h[k], ha);
  else rd128<(2 * HALF + k) * 5120 + (j - 1) * 1024>(f.w[k][j - 1], wa);
}
template <int I, int HALF, int N> __device__ __forceinline__ void g2_step(const G2Frag& c, G2Frag& nx, f32x4 (&acc)[5], unsigned wa, unsigned ha, bool mul) {
  g2_read1<I, HALF, N>(nx, wa, ha);
  if constexpr (N < 10) { if (mul) acc[N % 5] = MFMA(c.w[N / 5][N % 5], c.h[N / 5], acc[N % 5]); }
  SCHED();
  if constexpr (N < 11) g2_step<I, HALF, N + 1>(c, nx, acc, wa, ha, mul);
  else if (mul) asm volatile("" ::"v"(c.h[0]), "v"(c.h[1]), "v"(c.w[0][0]), "v"(c.w[0][1]), "v"(c.w[0][2]), "v"(c.w[0][3]), "v"(c.w[0][4]),
                             "v"(c.w[1][0]), "v"(c.w[1][1]), "v"(c.w[1][2]), "v"(c.w[1][3]), "v"(c.w[1][4]));
}
__device__ __forceinline__ void g2_wait(G2Frag& f) {
  asm volatile("s_waitcnt lgkmcnt(0)" : "+v"(f.h[0]), "+v"(f.h[1]), "+v"(f.w[0][0]), "+v"(f.w[0][1]), "+v"(f.w[0][2]), "+v"(f.w[0][3]), "+v"(f.w[0][4]),
               "+v"(f.w[1][0]), "+v"(f.w[1][1]), "+v"(f.w[1][2]), "+v"(f.w[1][3]), "+v"(f.w[1][4])::"memory");
}
struct G2Epi { u32x2 qv[5]; float gt; unsigned fg; };
template <int I> __device__ __forceinline__ void g2_iter(const Params& p, G2Frag& X, G2Frag& Y, f32x4 (&acc)[5], unsigned wa, unsigned ha, char* yflag, unsigned tag, int lane,
                                                         G2Epi& e, int prow, int cb8) {
  bar();
  if constexpr (I == 1) {
    wait_vm_for<0>(e.fg);
    if (yflag && lane == 0) st_dev(yflag, tag);
    if (!__all(e.fg == tag)) poll_flags(p.ws + WS_FLAGG + ((prow - (lane & 15)) >> 5) * 4, 1, tag, lane, p.ws);
    e.gt = __uint_as_float(ld_dev(p.ws + WS_GATE + prow * 4));
#pragma unroll
    for (int ct = 0; ct < 5; ++ct) e.qv[ct] = *(const u32x2*)(p.q + (size_t)prow * D + cb8 * 80 + ct * 16 + 4 * (lane >> 4));
  }
  const unsigned w = wa + g2_wslot(I % 3), hh = ha + g2_hslot(I);
  g2_step<I, 0, 0>(Y, X, acc, w, hh, I > 0);
  g2_wait(X);
  g2_step<I, 1, 0>(X, Y, acc, w, hh, true);
  g2_wait(Y);
  if constexpr (I < 11) g2_iter<I + 1>(p, X, Y, acc, wa, ha, yflag, tag, lane, e, prow, cb8);
}

__device__ __forceinline__ void g2_consumer(const Params& p, LAS char* lds, int row0, int cb8, int rt, int lane, unsigned tag, char* yflag) {
  const int g = lane >> 4, j = lane & 15;
  const int prow = row0 + rt * 16 + j;
  G2Epi e;
  e.fg = flag_issue(p.ws + WS_FLAGG + ((row0 + rt * 16) >> 5) * 4, 1, lane);
  const unsigned lb = (unsigned)(size_t)lds;
  f32x4 acc[5];
#pragma unroll
  for (int ct = 0; ct < 5; ++ct) acc[ct] = f32x4{0, 0, 0, 0};
  G2Frag X, Y;
  g2_iter<0>(p, X, Y, acc, lb + lane * 16, lb + rt * 4096 + lane * 16, yflag, tag, lane, e, prow, cb8);
#pragma unroll
  for (int n = 0; n < 10; ++n) acc[n % 5] = MFMA(Y.w[n / 5][n % 5], Y.h[n / 5], acc[n % 5]);
  const float gt = e.gt;
  const u32x2* qv = e.qv;
  const int qq = j >> 2, pp = lane & 3;
  const float omg = 1.f - gt;
#pragma unroll
  for (int ct = 0; ct < 5; ++ct) {
    const int yr = 16 * ct + 4 * g + qq, yc = 2 * rt + (pp >> 1);
    const v4i16 yv = __builtin_amdgcn_ds_read_tr16_b64_v4i16((LAS v4i16*)(lds + G2_YT + yr * 128 + ((yc ^ (((yr >> 1) & 3) << 1)) << 4) + 8 * (pp & 1)));
    const u32x2 y2 = __builtin_bit_cast(u32x2, yv);
    const f32x4 b = acc[ct];
    u32x2 w;
    w[0] = pk(gt * (blo(qv[ct][0]) * blo(y2[0])) + omg * b[0], gt * (bhi(qv[ct][0]) * bhi(y2[0])) + omg * b[1]);
    w[1] = pk(gt * (blo(qv[ct][1]) * blo(y2[1])) + omg * b[2], gt * (bhi(qv[ct][1]) * bhi(y2[1])) + omg * b[3]);
    *(u32x2*)(p.out + (size_t)prow * D + cb8 * 80 + ct * 16 + 4 * g) = w;
  }
}

__global__ __launch_bounds__(NTHR, 2) void gm_kernel(Params p) {
  extern __shared__ __attribute__((aligned(16))) char shm[];
  LAS char* lds = (LAS char*)shm;
  const int tid = threadIdx.x, wid = __builtin_amdgcn_readfirstlane(tid >> 6), lane = tid & 63;
  const int b = blockIdx.x, xcd = b & 7, r = b >> 3;
  STAMP(0);
  unsigned pf0 = 0, pf1 = 0;
  if (wid == 1) {
    unsigned long long pc; asm volatile("s_getpc_b64 %0" : "=s"(pc));
    const char* a = (const char*)(pc & ~127ull) + (r * 12 + (lane < 12 ? lane : 11)) * 128;
    asm volatile("global_load_dword %0, %1, off" : "=v"(pf0) : "v"(a) : "memory");
    const char* t = lane < 40 ? p.ws + lane * (WS_END / 40) : (lane < 48 ? (const char*)p.out + (lane - 40) * (S * D * 2 / 8) : (lane < 56 ? (const char*)p.hk + (lane - 48) * (D * S * 2 / 8) : (const char*)p.wd + (lane - 56) * (F * D * 2 / 8)));
    if (PFTLB) asm volatile("global_load_dword %0, %1, off" : "=v"(pf1) : "v"(t) : "memory");
  }
  unsigned epoch;
  asm volatile("s_load_dword %0, %1, 0x0" : "=s"(epoch) : "s"(p.ws) : "memory");

  const bool is_g1 = r < 24;
  const int rg = r & 3, cs = r >> 2;
  const bool conv = r < 20;
  const int ch0 = xcd * 80 + r * 4;
  const int row0 = (xcd >> 2) * 1024 + rg * 256, cb = 6 * (xcd & 3) + cs;
  if (is_g1 && wid >= 4) {
    const G1Off off = g1_offsets(lane, wid - 4);
    g1_issue<8>(p, lds, 0, row0, cb, wid - 4, off); g1_issue<8>(p, lds + G1_SLOT, 1, row0, cb, wid - 4, off);
  }
  asm volatile("s_waitcnt lgkmcnt(0)" : "+s"(epoch)::"memory");
  const unsigned tag = epoch + 1u;
  if (tid == 0) st_dev(p.ws + WS_ARRIVE + b * 4, tag);
  if (tid < 16) lds_signal(lds, tid, tag - 1u);
  STAMP(1);
  char* hflag = p.ws + WS_FLAGH + ((xcd >> 2) * 4 + rg) * 512 + (cb * 4 + wid) * 4;
  if (is_g1) {
    if (wid >= 4) {
      g1_loader<8>(p, lds, row0, cb, conv, ch0, wid - 4, lane, tag);
      STAMP(2);
    }
    else g1_consumer<8>(p, lds, row0, cb, wid, lane);
  } else {
    const int sidx = xcd * 8 + (r - 24);
    if (PDELAY) __builtin_amdgcn_s_sleep(PDELAY);
    if (wid < 4) {
      PTile t;
      p_load(p, t, sidx * 5 + wid, lane);
      p_finish(p, t, sidx * 5 + wid, lane, tag);
    } else if (wid < 6) {
      PHalf t;
      const int tile = sidx * 5 + 4;
      ph_load(p, t, tile, wid - 4, lane);
      ph_finish(p, t, tile, wid - 4, lane);
      wait_vm<0>();
      if (wid == 5) lds_signal(lds, 8, tag);
      else { lds_await(lds, 8, tag); if (lane == 0) st_dev(p.ws + WS_FLAGP + (tile >> 5) * 128 + (tile & 31) * 4, tag); }
    } else {
      const int u0 = (sidx * 2 + (wid - 6)) * 3;
#pragma unroll
      for (int u = 0; u < 3; ++u) repack_issue(p, lds + ((wid - 6) * 3 + u) * 5120, (u0 + u) >> 3, (u0 + u) & 7, lane);
#pragma unroll
      for (int u = 0; u < 3; ++u) repack_finish(p, lds + ((wid - 6) * 3 + u) * 5120, (u0 + u) >> 3, (u0 + u) & 7, lane);
      wait_vm<0>();
      if (lane == 0) st_dev(p.ws + WS_FLAGW + (sidx * 2 + (wid - 6)) * 4, tag);
      aux_gate(p, xcd * 256 + (r - 24) * 32 + (wid - 6) * 16, lane);
      wait_vm<0>();
      if (wid == 7) lds_signal(lds, 9, tag);
      else { lds_await(lds, 9, tag); if (lane == 0) st_dev(p.ws + WS_FLAGG + sidx * 4, tag); }
    }
    STAMP(2);
    if (b == 255 && wid == 1) {
      for (int i = 0; i < 4; ++i) poll_flags(p.ws + WS_ARRIVE + i * 256, 64, tag, lane, p.ws);
      if (lane == 0) st_dev(p.ws + WS_EPOCH, tag);
    }
  }
  STAMP(3);
  const int cb8 = r >> 2, orow0 = xcd * 256 + (r & 3) * 64;
  if (wid >= 4) {
    wait_vm<0>();
    bar();
    const int lw = wid - 4;
    if (lw == 0) { poll_flags2(p.ws + WS_FLAGW, 64, p.ws + WS_FLAGW + 256, 64, tag, lane, p.ws); lds_signal(lds, 1, tag); }
    else lds_await(lds, 1, tag);
    STAMP(7);
    const rsrc_t rw = mkrs(p.ws, WS_END);
#pragma unroll
    for (int st = 0; st < 2; ++st) g2_issue_w(rw, WS_WDP + cb8 * 48 * 5120, lane * 16, lds, st, lw);
    const char* w = p.ws + WS_WDP + (size_t)cb8 * 48 * 5120 + 2 * 20480 + ((r & 3) * 400 + lw * 100) * 128;
    unsigned d0, d1;
    asm volatile("global_load_dword %0, %1, off sc1" : "=v"(d0) : "v"(w + lane * 128) : "memory");
    asm volatile("global_load_dword %0, %1, off sc1" : "=v"(d1) : "v"(w + (64 + (lane < 36 ? lane : 35)) * 128) : "memory");
    STAMP(8);
    if (lw == 0) {
      const char* f0 = p.ws + WS_FLAGH + (orow0 >> 8) * 512;
      poll_flags2(f0, 64, f0 + 256, 32, tag, lane, p.ws);
      lds_signal(lds, 2, tag);
    } else lds_await(lds, 2, tag);
    asm volatile("s_waitcnt vmcnt(0)" : "+v"(d0), "+v"(d1)::"memory");
    STAMP(9);
    const unsigned hsrc = WS_H + ((orow0 >> 4) + lw) * 48 * 1024;
    g2_issue_h(rw, hsrc, lane * 16, lds, 0, lw); g2_issue_h(rw, hsrc, lane * 16, lds, 1, lw);
    if (conv) { lds_await(lds, 4, tag); lds_await(lds, 5, tag); lds_await(lds, 6, tag); lds_await(lds, 7, tag); }
    g2_issue_h(rw, hsrc, lane * 16, lds, 2, lw); g2_issue_h(rw, hsrc, lane * 16, lds, 3, lw);
  } else {
    if (is_g1) { wait_vm<0>(); if (lane == 0) st_dev(hflag, tag); }
    bar();
    if (conv) conv_channel(p, lds, ch0 + wid, wid, lane, tag);
  }
  STAMP(4);
  bar();
  STAMP(5);
  if (wid < 4) g2_consumer(p, lds, orow0, cb8, wid, lane, tag, conv ? p.ws + WS_FLAGY + ((ch0 + wid) / 80) * 512 + ((ch0 + wid) % 80) * 4 : nullptr);
  else g2_loader(p, lds, orow0, cb8, wid - 4, lane, tag);
  STAMP(6);
  asm volatile("s_waitcnt vmcnt(0)" : "+v"(pf0), "+v"(pf1)::"memory");
}
}

extern "C" void kernel_launch(void* const* d_in, const int* in_sizes, int n_in, void* d_out, int out_size, void* d_ws, size_t ws_size, hipStream_t stream) {
  using namespace gm;
  Params p;
  p.q = (const u16*)d_in[0]; p.k = (const u16*)d_in[1]; p.v = (const u16*)d_in[2]; p.sk = (const u16*)d_in[3];
  p.hk = (const u16*)d_in[4]; p.dv = (const u16*)d_in[5]; p.wg = (const u16*)d_in[7]; p.wu = (const u16*)d_in[8];
  p.wd = (const u16*)d_in[9]; p.gw = (const u16*)d_in[10]; p.out = (u16*)d_out; p.ws = (char*)d_ws;
  (void)hipFuncSetAttribute((const void*)gm_kernel, hipFuncAttributeMaxDynamicSharedMemorySize, LDS_BYTES);
  gm_kernel<<<NWG, NTHR, LDS_BYTES, stream>>>(p);
}
```

```cpp
#include <hip/hip_runtime.h>
#include <stdint.h>

namespace gm {
#define LAS __attribute__((address_space(3)))
typedef unsigned short u16;
typedef short bf16x8 __attribute__((ext_vector_type(8)));
typedef short v4i16 __attribute__((ext_vector_type(4)));
typedef float f32x4 __attribute__((ext_vector_type(4)));
typedef float f32x16 __attribute__((ext_vector_type(16)));
typedef float f32x2 __attribute__((ext_vector_type(2)));
typedef __bf16 bf16x2_t __attribute__((ext_vector_type(2)));
typedef unsigned u32x4 __attribute__((ext_vector_type(4)));
typedef unsigned u32x2 __attribute__((ext_vector_type(2)));

constexpr int S = 2048, D = 640, F = 1536;
constexpr int NWG = 256, NTHR = 512;
constexpr int SC1 = 16, NT = 2;
constexpr int LDS_BYTES = 163840;

constexpr unsigned WS_EPOCH = 0, WS_ARRIVE = 1024, WS_FLAGP = 4096, WS_FLAGH = 8192, WS_FLAGW = 20480, WS_FLAGG = 15360,
                   WS_FLAGY = 16384, WS_TMO = 24576, WS_GATE = 32768;
constexpr unsigned WS_KVR = 1u << 20, WS_YT = 4u << 20, WS_WDP = 8u << 20, WS_H = 12u << 20, WS_END = 20u << 20;

constexpr int G1_SLOT = 49152, G1_WG = 32768, G1_WU = 40960;
constexpr int C_H = 98304, C_HSTR = 9216, C_H2 = 4640;
constexpr int C_K = 64448, C_KSTR = 8464, C_K1 = 4288;
constexpr int L_PF = 163072;
constexpr int L_SIG = 163776;
constexpr int G2_YT = 84992;
__host__ __device__ constexpr int g2_hslot(int i) { return (i & 3) == 0 ? 40960 : ((i & 3) == 1 ? 135168 : ((i & 3) == 2 ? 98304 : 114688)); }
__host__ __device__ constexpr int g2_wslot(int i) { return i < 2 ? i * 20480 : 64512; }

struct Params {
  const u16 *q, *k, *v, *sk, *hk, *dv, *wg, *wu, *wd, *gw;
  u16* out;
  char* ws;
};

#define RLXA __ATOMIC_RELAXED, __HIP_MEMORY_SCOPE_AGENT
__device__ __forceinline__ unsigned ld_dev(const void* p) { return __hip_atomic_load((const unsigned*)p, RLXA); }
__device__ __forceinline__ void st_dev(void* p, unsigned v) { __hip_atomic_store((unsigned*)p, v, RLXA); }
__device__ __forceinline__ unsigned pk(float lo, float hi) { f32x2 v = {lo, hi}; return __builtin_bit_cast(unsigned, __builtin_convertvector(v, bf16x2_t)); }
__device__ __forceinline__ float blo(unsigned x) { return __uint_as_float(x << 16); }
__device__ __forceinline__ float bhi(unsigned x) { return __uint_as_float(x & 0xffff0000u); }
__device__ __forceinline__ float rb(float x) { return blo(pk(x, x)); }
template <int N> __device__ __forceinline__ void wait_vm() { asm volatile("s_waitcnt vmcnt(%0)" ::"n"(N) : "memory"); }
__device__ __forceinline__ void wait_lgkm0() { asm volatile("s_waitcnt lgkmcnt(0)" ::: "memory"); }
__device__ __forceinline__ void bar() { asm volatile("" ::: "memory"); __builtin_amdgcn_s_barrier(); asm volatile("" ::: "memory"); }
typedef __amdgpu_buffer_rsrc_t rsrc_t;
__device__ __forceinline__ rsrc_t mkrs(const void* base, unsigned bytes) { return __builtin_amdgcn_make_buffer_rsrc((void*)base, 0, bytes, 0x00020000); }
template <int AUX> __device__ __forceinline__ void bdma(rsrc_t rs, LAS char* l, unsigned voff, unsigned soff) {
  __builtin_amdgcn_raw_ptr_buffer_load_lds(rs, (LAS void*)l, 16, voff, soff, 0, AUX);
}
template <int OFF> __device__ __forceinline__ void rd128(bf16x8& d, unsigned a) { asm volatile("ds_read_b128 %0, %1 offset:%2" : "=v"(d) : "v"(a), "n"(OFF)); }
template <int OFF> __device__ __forceinline__ void rd64(u32x2& d, unsigned a) { asm volatile("ds_read_b64 %0, %1 offset:%2" : "=v"(d) : "v"(a), "n"(OFF)); }
template <int OFF> __device__ __forceinline__ void rd2x64(bf16x8& d, unsigned a) { asm volatile("ds_read2_b64 %0, %1 offset0:%2 offset1:%3" : "=v"(d) : "v"(a), "n"(OFF / 8), "n"(OFF / 8 + 1)); }
template <int OFF> __device__ __forceinline__ void rdtr(u32x2& d, unsigned a) { asm volatile("ds_read_b64_tr_b16 %0, %1 offset:%2" : "=v"(d) : "v"(a), "n"(OFF)); }
__device__ __forceinline__ bf16x8 cat(const u32x2& lo, const u32x2& hi) { return __builtin_bit_cast(bf16x8, u32x4{lo[0], lo[1], hi[0], hi[1]}); }
#define SCHED() __builtin_amdgcn_sched_barrier(0)
__device__ __forceinline__ void wait_vm0_seen() { __builtin_amdgcn_s_waitcnt(0x0F70); }
#define MFMA(a, b, c) __builtin_amdgcn_mfma_f32_16x16x32_bf16(a, b, c, 0, 0, 0)
#define MFMA32(a, b, c) __builtin_amdgcn_mfma_f32_32x32x16_bf16(a, b, c, 0, 0, 0)

__device__ __forceinline__ void poll_flags(const char* flags, int n, unsigned tag, int lane, char* ws) {
#pragma unroll 1
  for (unsigned spins = 0;; ++spins) {
    unsigned f = tag;
    if (lane < n) f = ld_dev(flags + lane * 4);
    if (__all(f == tag)) break;
    if (spins > (1u << 16)) { if (lane == 0) st_dev(ws + WS_TMO, tag); break; }
    __builtin_amdgcn_s_sleep(1);
  }
}

__device__ __forceinline__ void lds_signal(LAS char* lds, int i, unsigned v) { *(volatile LAS unsigned*)(lds + L_SIG + 4 * i) = v; }
__device__ __forceinline__ void lds_await(LAS char* lds, int i, unsigned v) {
#pragma unroll 1
  for (unsigned n = 0; *(volatile LAS unsigned*)(lds + L_SIG + 4 * i) != v && n < (1u << 22); ++n) __builtin_amdgcn_s_sleep(1);
}
__device__ __forceinline__ void poll_flags2(const char* fa, int na, const char* fb, int nb, unsigned tag, int lane, char* ws) {
#pragma unroll 1
  for (unsigned spins = 0;; ++spins) {
    unsigned f = tag, g = tag;
    if (lane < na) f = ld_dev(fa + lane * 4);
    if (lane < nb) g = ld_dev(fb + lane * 4);
    if (__all(f == tag && g == tag)) break;
    if (spins > (1u << 16)) { if (lane == 0) st_dev(ws + WS_TMO, tag); break; }
    __builtin_amdgcn_s_sleep(1);
  }
}
__device__ __forceinline__ unsigned flag_issue(const char* flags, int n, int lane) {
  unsigned f;
  const char* a = flags + (lane < n ? lane : 0) * 4;
  asm volatile("global_load_dword %0, %1, off sc1" : "=v"(f) : "v"(a) : "memory");
  return f;
}
template <int N> __device__ __forceinline__ void wait_vm_for(unsigned& f) { asm volatile("s_waitcnt vmcnt(%1)" : "+v"(f) : "n"(N) : "memory"); }
template <int N> __device__ __forceinline__ void wait_vm_for(unsigned& f, unsigned& g) { asm volatile("s_waitcnt vmcnt(%2)" : "+v"(f), "+v"(g) : "n"(N) : "memory"); }

constexpr int PM_BYTES = 17408;
__device__ __forceinline__ void pm_issue(const Params& p, LAS char* img, int tile, int lane) {
  const int s0 = (tile & 31) * 64, d0 = (tile >> 5) * 64;
  const rsrc_t rk = mkrs(p.k, S * D * 2), rv = mkrs(p.v, S * D * 2);
  const int rl = lane >> 3;
  const int vo = rl * (D * 2) + d0 * 2 + (((lane & 7) ^ rl) << 4);
#pragma unroll
  for (int j = 0; j < 9; ++j) bdma<NT>(rk, img + j * 1024, (unsigned)(vo + (s0 - 8 + 8 * j) * (D * 2)), 0);
#pragma unroll
  for (int j = 0; j < 8; ++j) bdma<NT>(rv, img + 9216 + j * 1024, (unsigned)(vo + (s0 + 8 * j) * (D * 2)), 0);
}
__device__ __forceinline__ void pm_finish(const Params& p, LAS char* img, int tile, int lane, unsigned tag) {
  const int s0 = (tile & 31) * 64, d0 = (tile >> 5) * 64;
  const int i = lane & 15, g = lane >> 4;
  u32x2 w[4];
#pragma unroll
  for (int ct = 0; ct < 4; ++ct) w[ct] = *(const u32x2*)(p.sk + (d0 + 16 * ct + i) * 4);
  const bool act = (i >> 3) == (g & 1);
  const int e = i & 7;
  bf16x8 bk[4][2], bv[2];
#pragma unroll
  for (int ct = 0; ct < 4; ++ct)
#pragma unroll
    for (int m = 0; m < 2; ++m) {
      const unsigned t = (g >> 1) ? (w[ct][m] >> 16) : (w[ct][m] & 0xffffu);
      const unsigned x = act ? (t << (16 * (e & 1))) : 0u;
      bk[ct][m] = __builtin_bit_cast(bf16x8, u32x4{(e >> 1) == 0 ? x : 0u, (e >> 1) == 1 ? x : 0u, (e >> 1) == 2 ? x : 0u, (e >> 1) == 3 ? x : 0u});
    }
#pragma unroll
  for (int par = 0; par < 2; ++par) {
    const unsigned x = (g == 2 * par + (i >> 3)) ? (0x3f80u << (16 * (e & 1))) : 0u;
    bv[par] = __builtin_bit_cast(bf16x8, u32x4{(e >> 1) == 0 ? x : 0u, (e >> 1) == 1 ? x : 0u, (e >> 1) == 2 ? x : 0u, (e >> 1) == 3 ? x : 0u});
  }
  const unsigned ib = (unsigned)(size_t)img;
  unsigned ka[2];
#pragma unroll
  for (int m = 0; m < 2; ++m) { const int t = 2 * m + (g >> 1); ka[m] = (8 + i - t) * 128 + (((g & 1) ^ ((i - t) & 7)) << 4); }
  const unsigned va = 9216 + i * 128 + ((g ^ (i & 7)) << 4);
  wait_vm0_seen();
  struct PF { bf16x8 k0[4], k1[4]; };
  PF F0, F1;
  bf16x8 vf[2][4];
  auto rdk = [&](PF& f, int ct) {
    const unsigned a0 = ib + (ka[0] ^ (ct << 5)), a1 = ib + (ka[1] ^ (ct << 5));
    rd128<0>(f.k0[0], a0); rd128<2048>(f.k0[1], a0); rd128<4096>(f.k0[2], a0); rd128<6144>(f.k0[3], a0);
    rd128<0>(f.k1[0], a1); rd128<2048>(f.k1[1], a1); rd128<4096>(f.k1[2], a1); rd128<6144>(f.k1[3], a1);
  };
  auto rdv = [&](bf16x8 (&v)[4], int cp) { const unsigned a = ib + (va ^ (cp << 6)); rd128<0>(v[0], a); rd128<2048>(v[1], a); rd128<4096>(v[2], a); rd128<6144>(v[3], a); };
  auto waitk = [&](PF& f) { asm volatile("s_waitcnt lgkmcnt(0)" : "+v"(f.k0[0]), "+v"(f.k0[1]), "+v"(f.k0[2]), "+v"(f.k0[3]), "+v"(f.k1[0]), "+v"(f.k1[1]), "+v"(f.k1[2]), "+v"(f.k1[3])::"memory"); };
  u32x2 o[4][4];
  auto mul = [&](const PF& f, const bf16x8 (&v)[4], int ct) {
    f32x4 ak[4], av[4];
#pragma unroll
    for (int pt = 0; pt < 4; ++pt) ak[pt] = MFMA(f.k0[pt], bk[ct][0], (f32x4{0, 0, 0, 0}));
#pragma unroll
    for (int pt = 0; pt < 4; ++pt) av[pt] = MFMA(v[pt], bv[ct & 1], (f32x4{0, 0, 0, 0}));
#pragma unroll
    for (int pt = 0; pt < 4; ++pt) ak[pt] = MFMA(f.k1[pt], bk[ct][1], ak[pt]);
#pragma unroll
    for (int pt = 0; pt < 4; ++pt) o[ct][pt] = u32x2{pk(ak[pt][3] * av[pt][3], ak[pt][2] * av[pt][2]), pk(ak[pt][1] * av[pt][1], ak[pt][0] * av[pt][0])};
  };
  rdv(vf[0], 0); rdk(F0, 0); rdv(vf[1], 1);
  waitk(F0);
  asm volatile("" : "+v"(vf[0][0]), "+v"(vf[0][1]), "+v"(vf[0][2]), "+v"(vf[0][3]), "+v"(vf[1][0]), "+v"(vf[1][1]), "+v"(vf[1][2]), "+v"(vf[1][3]));
  rdk(F1, 1); SCHED(); mul(F0, vf[0], 0); SCHED(); waitk(F1);
  rdk(F0, 2); SCHED(); mul(F1, vf[0], 1); SCHED(); waitk(F0);
  rdk(F1, 3); SCHED(); mul(F0, vf[1], 2); SCHED(); waitk(F1);
  mul(F1, vf[1], 3);
  __builtin_amdgcn_sched_barrier(0);
#pragma unroll
  for (int ct = 0; ct < 4; ++ct)
#pragma unroll
    for (int pt = 0; pt < 4; ++pt) *(LAS u32x2*)(img + (16 * ct + i) * 144 + 2 * (60 - 16 * pt - 4 * g)) = o[ct][pt];
  wait_lgkm0();
  __builtin_amdgcn_sched_barrier(0);
  auto rs = __builtin_amdgcn_make_buffer_rsrc(p.ws, 0, WS_END, 0x00020000);
#pragma unroll
  for (int j = 0; j < 8; ++j) {
    const int ch = 8 * j + (lane >> 3);
    __builtin_amdgcn_raw_buffer_store_b128(*(const LAS u32x4*)(img + ch * 144 + (lane & 7) * 16), rs, WS_KVR + ((d0 + ch) * S + (S - 64 - s0) + 8 * (lane & 7)) * 2, 0, SC1);
  }
  wait_vm<0>();
  if (lane == 0) st_dev(p.ws + WS_FLAGP + (tile >> 5) * 128 + (tile & 31) * 4, tag);
}

__device__ __forceinline__ void repack_issue(const Params& p, LAS char* img, int ks, int cb8, int lane) {
  const rsrc_t rd = mkrs(p.wd, F * D * 2);
#pragma unroll
  for (int j = 0; j < 5; ++j) {
    const int n = 64 * j + lane, row = n / 10, c = n - row * 10;
    bdma<NT>(rd, img + j * 1024, row * (D * 2) + c * 16, ks * 32 * (D * 2) + cb8 * 160);
  }
}
struct RpAddr { unsigned a0; unsigned o[5]; };
__device__ __forceinline__ RpAddr repack_addr(LAS char* img, int ks, int cb8, int lane) {
  const int g = lane >> 4, qq = (lane & 15) >> 2, pp = lane & 3;
  RpAddr r;
  r.a0 = (unsigned)(size_t)img + (16 * (g >> 1) + 4 * (g & 1) + qq) * 160 + 8 * pp;
#pragma unroll
  for (int ct = 0; ct < 5; ++ct) r.o[ct] = WS_WDP + (((cb8 * 48 + ks) * 5 + ct) * 64 + lane) * 16;
  return r;
}
__device__ __forceinline__ void repack_store(const Params& p, const RpAddr& r) {
  u32x2 lo[5], hi[5];
  rdtr<0>(lo[0], r.a0); rdtr<1280>(hi[0], r.a0); rdtr<32>(lo[1], r.a0); rdtr<1312>(hi[1], r.a0); rdtr<64>(lo[2], r.a0); rdtr<1344>(hi[2], r.a0);
  rdtr<96>(lo[3], r.a0); rdtr<1376>(hi[3], r.a0); rdtr<128>(lo[4], r.a0); rdtr<1408>(hi[4], r.a0);
  asm volatile("s_waitcnt lgkmcnt(0)" : "+v"(lo[0]), "+v"(lo[1]), "+v"(lo[2]), "+v"(lo[3]), "+v"(lo[4]), "+v"(hi[0]), "+v"(hi[1]), "+v"(hi[2]), "+v"(hi[3]), "+v"(hi[4])::"memory");
  auto rs = __builtin_amdgcn_make_buffer_rsrc(p.ws, 0, WS_END, 0x00020000);
#pragma unroll
  for (int ct = 0; ct < 5; ++ct) __builtin_amdgcn_raw_buffer_store_b128(u32x4{lo[ct][0], lo[ct][1], hi[ct][0], hi[ct][1]}, rs, r.o[ct], 0, SC1);
}
__device__ __forceinline__ void aux_gate(const Params& p, int row0, int lane) {
  const int r4 = lane >> 4, c16 = lane & 15;
  u32x4 gw[5], x[4][5];
#pragma unroll
  for (int i = 0; i < 5; ++i) {
    gw[i] = *(const u32x4*)(p.gw + (c16 + 16 * i) * 8);
#pragma unroll
    for (int it = 0; it < 4; ++it) x[it][i] = *(const u32x4*)(p.q + (size_t)(row0 + 4 * it + r4) * D + (c16 + 16 * i) * 8);
  }
#pragma unroll
  for (int it = 0; it < 4; ++it) {
    float z = 0.f;
#pragma unroll
    for (int i = 0; i < 5; ++i)
#pragma unroll
      for (int j = 0; j < 4; ++j) z += blo(x[it][i][j]) * blo(gw[i][j]) + bhi(x[it][i][j]) * bhi(gw[i][j]);
    z += __shfl_xor(z, 1); z += __shfl_xor(z, 2); z += __shfl_xor(z, 4); z += __shfl_xor(z, 8);
    z = rb(z);
    const float gt = rb(1.f / (1.f + __expf(-z)));
    if (c16 == 0) st_dev(p.ws + WS_GATE + (row0 + it * 4 + r4) * 4, __float_as_uint(gt));
  }
}

struct G1Off { unsigned x, g, u; };
__device__ __forceinline__ G1Off g1_offsets(int lane, int lw) {
  const int rl = lane >> 3, c = lane & 7;
  return G1Off{(unsigned)(rl * (D * 2) + ((c ^ ((4 * (lw & 1) + (rl >> 1)) & 7)) << 4)),
               (unsigned)(rl * (F * 2) + ((c ^ (((rl >> 1) & 1) << 2)) << 4)),
               (unsigned)(rl * (F * 2) + ((c ^ (((rl >> 1) & 1) << 2)) << 4))};
}
template <int NX>
__device__ __forceinline__ void g1_issue(const Params& p, LAS char* slot, int ks, int row0, int cb, int lw, const G1Off& o) {
  const rsrc_t rq = mkrs(p.q, S * D * 2), rg = mkrs(p.wg, D * F * 2), ru = mkrs(p.wu, D * F * 2);
  const unsigned xb = row0 * (D * 2) + ks * 128 + lw * (8 * D * 2);
  const unsigned wo = (64 * ks + 16 * lw) * (F * 2) + cb * 128;
  bdma<0>(rg, slot + G1_WG + (2 * lw) * 1024, o.g, wo);
  bdma<0>(ru, slot + G1_WU + (2 * lw) * 1024, o.u, wo);
  bdma<0>(rg, slot + G1_WG + (2 * lw + 1) * 1024, o.g, wo + 8 * (F * 2));
  bdma<0>(ru, slot + G1_WU + (2 * lw + 1) * 1024, o.u, wo + 8 * (F * 2));
#pragma unroll
  for (int i = 0; i < NX; ++i) bdma<0>(rq, slot + (lw + 4 * i) * 1024, o.x, xb + i * (32 * D * 2));
}

__device__ __forceinline__ void ldsw128(unsigned a, u32x4 v) { asm volatile("ds_write_b128 %0, %1" ::"v"(a), "v"(v) : "memory"); }
__device__ __forceinline__ void conv_issue_k(const Params& p, LAS char* lds, int ch, int lw, int lane) {
  const rsrc_t rw = mkrs(p.ws, WS_END);
  const unsigned so = WS_KVR + ch * (S * 2), vo = lane * 16;
  LAS char* d = lds + C_K + lw * C_KSTR;
#pragma unroll
  for (int j = 0; j < 4; ++j) bdma<SC1>(rw, d + 16 + C_K1 + j * 1024, vo, so + j * 1024);
#pragma unroll
  for (int j = 0; j < 4; ++j) bdma<SC1>(rw, d + 16 + j * 1024, vo, so + j * 1024 + 2);
  if (lane < 8) ldsw128((unsigned)(size_t)d + (lane >> 2) * C_K1 + (257 + (lane & 3)) * 16, u32x4{0, 0, 0, 0});
}
template <int NX>
__device__ __forceinline__ void g1_loader(const Params& p, LAS char* lds, int row0, int cb, bool conv, int ch0, int lw, int lane, unsigned& tag) {
  constexpr int NP = NX + 4;
  const G1Off off = g1_offsets(lane, lw);
  const int ch = min(ch0 + lw, D - 1);
  const char* touch = (const char*)p.hk + (size_t)ch * (S * 2) + (lane & 31) * 128;
  const char* fpa = p.ws + WS_FLAGP + (min(ch0, D - 4) >> 6) * 128 + (lane & 31) * 4;
  unsigned dummy, fp;
  wait_vm<NP>(); bar(); g1_issue<NX>(p, lds + 2 * G1_SLOT, 2, row0, cb, lw, off);
  tag = __builtin_amdgcn_readfirstlane(*(volatile LAS unsigned*)(lds + L_SIG + 60));
  fp = tag;
  wait_vm<NP>(); bar(); g1_issue<NX>(p, lds, 3, row0, cb, lw, off);
  wait_vm<NP>(); bar(); g1_issue<NX>(p, lds + G1_SLOT, 4, row0, cb, lw, off);
  wait_vm<NP>(); bar(); g1_issue<NX>(p, lds + 2 * G1_SLOT, 5, row0, cb, lw, off);
  wait_vm<NP>(); bar(); g1_issue<NX>(p, lds, 6, row0, cb, lw, off);
  wait_vm<NP>(); bar(); g1_issue<NX>(p, lds + G1_SLOT, 7, row0, cb, lw, off);
  asm volatile("global_load_dword %0, %1, off" : "=v"(dummy) : "v"(touch) : "memory");
  wait_vm<NP + 1>(); bar(); g1_issue<NX>(p, lds + 2 * G1_SLOT, 8, row0, cb, lw, off);
  wait_vm<NP + 1>(); bar();
  if (conv && lw == 0) asm volatile("global_load_dword %0, %1, off sc1" : "=v"(fp) : "v"(fpa) : "memory");
  g1_issue<NX>(p, lds, 9, row0, cb, lw, off);
  asm volatile("s_waitcnt vmcnt(%2)" : "+v"(fp), "+v"(dummy) : "n"(NP) : "memory");
  if (conv && lw == 0 && __all(fp == tag)) lds_signal(lds, 0, tag);
  wait_lgkm0();
  bar();
  const bool early = conv && *(volatile LAS unsigned*)(lds + L_SIG) == tag;
  if (early) { conv_issue_k(p, lds, ch, lw, lane); wait_vm<8>(); } else wait_vm<0>();
  wait_lgkm0();
  bar();
  if (conv) {
    const rsrc_t rh = mkrs(p.hk, D * S * 2);
    const unsigned so = ch * (S * 2), vo = lane * 16, vz = lane ? lane * 16 - 4 : 0;
    LAS char* d = lds + C_H + lw * C_HSTR;
    bdma<0>(rh, d + 448, vz, so);
#pragma unroll
    for (int j = 1; j < 4; ++j) bdma<0>(rh, d + 448 + j * 1024, vo, so + j * 1024 - 4);
#pragma unroll
    for (int j = 0; j < 4; ++j) bdma<0>(rh, d + 448 + C_H2 + j * 1024, vo, so + j * 1024);
    if (lane < 56) ldsw128((unsigned)(size_t)d + (lane >= 28 ? C_H2 + (lane - 28) * 16 : lane * 16), u32x4{0, 0, 0, 0});
    if (!early) {
      if (lw == 0) { poll_flags(p.ws + WS_FLAGP + (ch0 >> 6) * 128, 32, tag, lane, p.ws); lds_signal(lds, 3, tag); }
      else lds_await(lds, 3, tag);
      conv_issue_k(p, lds, ch, lw, lane);
    }
    wait_lgkm0();
  }
}

struct G1Frag { u32x2 gl, gh, ul, uh; bf16x8 x[4]; };
template <int KK, int I>
__device__ __forceinline__ void g1_read1(G1Frag& f, unsigned wa, unsigned xa) {
  if constexpr (I == 0) rdtr<KK * 2048>(f.gl, wa);
  else if constexpr (I == 1) rdtr<KK * 2048 + 512>(f.gh, wa);
  else if constexpr (I == 2) rd128<0>(f.x[0], xa);
  else if constexpr (I == 3) rdtr<KK * 2048 + 8192>(f.ul, wa);
  else if constexpr (I == 4) rdtr<KK * 2048 + 8192 + 512>(f.uh, wa);
  else rd128<(I - 4) * 4096>(f.x[I - 4], xa);
}
template <int KK, int N, bool LAST>
__device__ __forceinline__ void g1_step(G1Frag& c, G1Frag& nx, f32x16 (&ag)[4], f32x16 (&au)[4], unsigned wa, unsigned xa) {
  constexpr int PER = LAST ? 2 : 1;
  if constexpr (N == 0) asm volatile("s_waitcnt lgkmcnt(3)" : "+v"(c.gl), "+v"(c.gh), "+v"(c.ul), "+v"(c.uh), "+v"(c.x[0]));
  if constexpr (N == 2) asm volatile("s_waitcnt lgkmcnt(%1)" : "+v"(c.x[1]) : "n"(2 + 2 * PER));
  if constexpr (N == 4) asm volatile("s_waitcnt lgkmcnt(%1)" : "+v"(c.x[2]) : "n"(1 + 4 * PER));
  if constexpr (N == 6) asm volatile("s_waitcnt lgkmcnt(%1)" : "+v"(c.x[3]) : "n"(6 * PER < 8 ? 6 * PER : 8));
  if constexpr (PER * N < 8) g1_read1<KK, PER * N>(nx, wa, xa);
  if constexpr (PER == 2 && PER * N + 1 < 8) g1_read1<KK, PER * N + 1>(nx, wa, xa);
  if constexpr ((N & 1) == 0) ag[N / 2] = MFMA32(cat(c.gl, c.gh), c.x[N / 2], ag[N / 2]);
  else au[N / 2] = MFMA32(cat(c.ul, c.uh), c.x[N / 2], au[N / 2]);
  SCHED();
  if constexpr (N + 1 < 8) g1_step<KK, N + 1, LAST>(c, nx, ag, au, wa, xa);
  else
    asm volatile("" ::"v"(c.gl), "v"(c.gh), "v"(c.ul), "v"(c.uh), "v"(c.x[0]), "v"(c.x[1]), "v"(c.x[2]), "v"(c.x[3]));
}

template <int NRT>
__device__ __forceinline__ void g1_consumer(const Params& p, LAS char* lds, int row0, int cb, int w, int lane) {
  const int cg = w & 1, rh = w >> 1;
  const int j = lane & 31, h = lane >> 5, fh = (lane >> 4) & 1, qq = (lane & 15) >> 2, pp = lane & 3;
  const unsigned lb = (unsigned)(size_t)lds;
  const unsigned xl = rh * 16384 + j * 128 + ((h ^ ((j >> 1) & 7)) << 4);
  const unsigned wa0 = lb + G1_WG + (8 * h + qq) * 128 + (((4 * cg + 2 * fh + (pp >> 1)) ^ (((qq >> 1) & 1) << 2)) << 4) + 8 * (pp & 1);
  f32x16 ag[4], au[4];
  G1Frag A, B;
#pragma unroll
  for (int m = 0; m < 4; ++m) {
#pragma unroll
    for (int e = 0; e < 16; ++e) { ag[m][e] = 0.f; au[m][e] = 0.f; }
    B.x[m] = bf16x8{0, 0, 0, 0, 0, 0, 0, 0};
  }
  B.gl = u32x2{0, 0}; B.gh = B.gl; B.ul = B.gl; B.uh = B.gl;
  int slot = 0;
#pragma unroll 1
  for (int s = 0; s < 10; ++s) {
    bar();
    const unsigned wa = wa0 + slot, xb = lb + slot;
    g1_step<0, 0, false>(B, A, ag, au, wa, xb + xl);
    g1_step<1, 0, false>(A, B, ag, au, wa, xb + (xl ^ 32));
    g1_step<2, 0, false>(B, A, ag, au, wa, xb + (xl ^ 64));
    g1_step<3, 0, true>(A, B, ag, au, wa, xb + (xl ^ 96));
    asm volatile("s_waitcnt lgkmcnt(0)" : "+v"(B.gl), "+v"(B.gh), "+v"(B.ul), "+v"(B.uh), "+v"(B.x[0]), "+v"(B.x[1]), "+v"(B.x[2]), "+v"(B.x[3])::"memory");
    slot = slot == 2 * G1_SLOT ? 0 : slot + G1_SLOT;
  }
#pragma unroll
  for (int m = 0; m < 4; ++m) { ag[m] = MFMA32(cat(B.gl, B.gh), B.x[m], ag[m]); au[m] = MFMA32(cat(B.ul, B.uh), B.x[m], au[m]); }
  auto rs = __builtin_amdgcn_make_buffer_rsrc(p.ws, 0, WS_END, 0x00020000);
#pragma unroll
  for (int m = 0; m < 4; ++m) {
    const int rtile = (row0 >> 4) + 2 * (4 * rh + m) + (j >> 4);
#pragma unroll
    for (int c = 0; c < 2; ++c) {
      u32x4 o;
#pragma unroll
      for (int e = 0; e < 4; ++e) o[e] = pk(ag[m][8 * c + 2 * e] * au[m][8 * c + 2 * e], ag[m][8 * c + 2 * e + 1] * au[m][8 * c + 2 * e + 1]);
      __builtin_amdgcn_raw_buffer_store_b128(o, rs, WS_H + ((rtile * 48 + 2 * cb + cg) * 64 + (2 * c + h) * 16 + (j & 15)) * 16, 0, SC1);
    }
  }
}

struct CSet { bf16x8 a[8], b[8]; };
template <int R, int K> __device__ __forceinline__ void conv_read1(CSet& s, unsigned A0, unsigned B0, unsigned B1) {
  if constexpr (K < 8) rd128<64 * R + 512 * K>(s.a[K], A0);
  else if constexpr (7 - (K - 8) < 4) rd2x64<64 * R + 512 * (7 - (K - 8))>(s.b[K - 8], B0);
  else rd2x64<64 * R + 512 * (7 - (K - 8)) - 2048>(s.b[K - 8], B1);
}
template <int R, int K> __device__ __forceinline__ void conv_reads(CSet& s, unsigned A0, unsigned B0, unsigned B1) {
  conv_read1<R, K>(s, A0, B0, B1);
  if constexpr (K < 15) conv_reads<R, K + 1>(s, A0, B0, B1);
}
__device__ __forceinline__ void conv_wait(CSet& s) {
  asm volatile("s_waitcnt lgkmcnt(0)" : "+v"(s.a[0]), "+v"(s.a[1]), "+v"(s.a[2]), "+v"(s.a[3]), "+v"(s.a[4]), "+v"(s.a[5]), "+v"(s.a[6]), "+v"(s.a[7]),
               "+v"(s.b[0]), "+v"(s.b[1]), "+v"(s.b[2]), "+v"(s.b[3]), "+v"(s.b[4]), "+v"(s.b[5]), "+v"(s.b[6]), "+v"(s.b[7])::"memory");
}
constexpr int conv_pa(int n) { int a = 0; while (n >= 8 - a) { n -= 8 - a; ++a; } return a; }
constexpr int conv_pe(int n) { int a = 0; while (n >= 8 - a) { n -= 8 - a; ++a; } return n; }
struct CKv { u32x2 l[8], h[8]; };
template <int R, int N> __device__ __forceinline__ void conv_step(const CSet& c, CSet& nx, CKv& kv, f32x4 (&acc)[8], unsigned A0, unsigned B0, unsigned B1) {
  if constexpr (R < 7 && N < 32 && (N & 1) == 0) conv_read1<R + 1, ((N / 2) & 1) ? 8 + N / 4 : N / 4>(nx, A0, B0, B1);
  if constexpr (R == 7 && N < 16) {
    if constexpr (N & 1) rd64<512 * (7 - N / 2)>(kv.h[N / 2], B0); else rd64<512 * (7 - N / 2) + 64>(kv.l[N / 2], B0);
  }
  constexpr int a = conv_pa(N), e = conv_pe(N);
  acc[a + e] = MFMA(c.a[a], c.b[e], acc[a + e]);
  SCHED();
  if constexpr (N < 35) conv_step<R, N + 1>(c, nx, kv, acc, A0, B0, B1);
  else asm volatile("" ::"v"(c.a[0]), "v"(c.a[1]), "v"(c.a[2]), "v"(c.a[3]), "v"(c.a[4]), "v"(c.a[5]), "v"(c.a[6]), "v"(c.a[7]),
                    "v"(c.b[0]), "v"(c.b[1]), "v"(c.b[2]), "v"(c.b[3]), "v"(c.b[4]), "v"(c.b[5]), "v"(c.b[6]), "v"(c.b[7]));
}
template <int R> __device__ __forceinline__ void conv_res(const CSet& c, CSet& nx, CKv& kv, f32x4 (&acc)[8], unsigned A0, unsigned B0, unsigned B1) {
  conv_step<R, 0>(c, nx, kv, acc, A0, B0, B1);
  if constexpr (R < 7) conv_wait(nx);
  else {
    asm volatile("s_waitcnt lgkmcnt(0)" : "+v"(kv.l[0]), "+v"(kv.l[1]), "+v"(kv.l[2]), "+v"(kv.l[3]), "+v"(kv.l[4]), "+v"(kv.l[5]), "+v"(kv.l[6]), "+v"(kv.l[7]),
                 "+v"(kv.h[0]), "+v"(kv.h[1]), "+v"(kv.h[2]), "+v"(kv.h[3]), "+v"(kv.h[4]), "+v"(kv.h[5]), "+v"(kv.h[6]), "+v"(kv.h[7])::"memory");
  }
}

__device__ __forceinline__ void conv_channel(const Params& p, LAS char* lds, int ch, int c, int lane, unsigned tag) {
  LAS char* H = lds + C_H + c * C_HSTR;
  LAS char* K = lds + C_K + c * C_KSTR;
  {
    if (lane == 0) {
      const u32x4 t = *(LAS u32x4*)(H + C_H2 + 448);
      *(LAS u32x4*)(H + 448) = u32x4{0u, t[0], t[1], t[2]};
      *(LAS u16*)(K + 14) = *(LAS u16*)(K + C_K1 + 16);
      *(LAS u16*)(K + 2055 * 2) = 0;
    }
    wait_lgkm0();
    __builtin_amdgcn_sched_barrier(0);
  }
  const unsigned dw = ((const __attribute__((address_space(4))) unsigned*)(size_t)p.dv)[ch >> 1];
  const float dd = (ch & 1) ? bhi(dw) : blo(dw);
  const int rc = lane & 15, g = lane >> 4;
  const unsigned A0 = (unsigned)(size_t)H + (rc & 1) * C_H2 + 64 * (rc >> 1) + 16 * g;
  const unsigned B0 = (unsigned)(size_t)K + (rc & 1) * C_K1 + 72 - 8 * (rc >> 1) + 16 * g;
  f32x4 acc[8];
#pragma unroll
  for (int t = 0; t < 8; ++t) acc[t] = f32x4{0, 0, 0, 0};
  const unsigned B1 = B0 + 2048;
  CSet X, Y2;
  CKv kv;
  conv_reads<0, 0>(X, A0, B0, B1);
  conv_wait(X);
  conv_res<0>(X, Y2, kv, acc, A0, B0, B1);
  conv_res<1>(Y2, X, kv, acc, A0, B0, B1);
  conv_res<2>(X, Y2, kv, acc, A0, B0, B1);
  conv_res<3>(Y2, X, kv, acc, A0, B0, B1);
  conv_res<4>(X, Y2, kv, acc, A0, B0, B1);
  conv_res<5>(Y2, X, kv, acc, A0, B0, B1);
  conv_res<6>(X, Y2, kv, acc, A0, B0, B1);
  const unsigned KB = (unsigned)(size_t)K + (rc & 1) * C_K1 + 2 * (2052 - 64 * g - 4 * (rc >> 1) - 32 - 1792);
  conv_res<7>(Y2, X, kv, acc, A0, KB, KB);
  lds_signal(lds, 4 + c, tag);
  LAS char* Y = K;
  const bool odd = rc & 1;
#pragma unroll
  for (int t = 0; t < 8; ++t) {
    const float v0 = acc[t][0] + blo(kv.l[t][1]) * dd, v1 = acc[t][1] + blo(kv.l[t][0]) * dd;
    const float v2 = acc[t][2] + blo(kv.h[t][1]) * dd, v3 = acc[t][3] + blo(kv.h[t][0]) * dd;
    const unsigned x01 = pk(v0, v1), x23 = pk(v2, v3);
    const unsigned rcv = (unsigned)__builtin_amdgcn_mov_dpp((int)(odd ? x01 : x23), 0xB1, 0xF, 0xF, true);
    const unsigned a = odd ? rcv : x01, bb = odd ? x23 : rcv;
    *(LAS u32x2*)(Y + 2 * (256 * t + 64 * g + 32 * (rc & 1) + 4 * (rc >> 1))) = u32x2{__builtin_amdgcn_perm(bb, a, 0x05040100u), __builtin_amdgcn_perm(bb, a, 0x07060302u)};
  }
  wait_lgkm0();
  __builtin_amdgcn_sched_barrier(0);
  auto rs = __builtin_amdgcn_make_buffer_rsrc(p.ws, 0, WS_END, 0x00020000);
#pragma unroll
  for (int it = 0; it < 4; ++it) {
    const int pos = 8 * (lane + 64 * it);
    __builtin_amdgcn_raw_buffer_store_b128(*(const LAS u32x4*)(Y + 2 * pos), rs, WS_YT + (ch * S + pos) * 2, 0, SC1);
  }
}

__device__ __forceinline__ void g2_issue_w(rsrc_t rw, unsigned wsrc, unsigned vo, LAS char* lds, int i, int lw) {
  LAS char* d = lds + g2_wslot(i % 3);
#pragma unroll
  for (int j = 0; j < 5; ++j) bdma<SC1>(rw, d + (lw * 5 + j) * 1024, vo, wsrc + i * 20480 + (lw * 5 + j) * 1024);
}
__device__ __forceinline__ void g2_issue_h(rsrc_t rw, unsigned hsrc, unsigned vo, LAS char* lds, int i, int lw) {
#pragma unroll
  for (int j = 0; j < 4; ++j) bdma<SC1>(rw, lds + g2_hslot(i) + lw * 4096 + j * 1024, vo, hsrc + i * 4096 + j * 1024);
}
__device__ __forceinline__ void g2_loader(const Params& p, LAS char* lds, int row0, int cb8, int lw, int lane, unsigned tag) {
  const rsrc_t rw = mkrs(p.ws, WS_END);
  const unsigned hsrc = WS_H + ((row0 >> 4) + lw) * 48 * 1024, wsrc = WS_WDP + cb8 * 48 * 5120, vo = lane * 16;
  unsigned fy0 = tag, fy1 = tag;
  g2_issue_w(rw, wsrc, vo, lds, 2, lw);
  wait_vm<17>(); bar();
  wait_vm<13>(); bar();
  g2_issue_w(rw, wsrc, vo, lds, 3, lw); g2_issue_h(rw, hsrc, vo, lds, 4, lw);
  wait_vm<9>(); bar();
  g2_issue_w(rw, wsrc, vo, lds, 4, lw); g2_issue_h(rw, hsrc, vo, lds, 5, lw);
#pragma unroll 1
  for (int i = 3; i <= 6; ++i) {
    wait_vm<13>(); bar();
    g2_issue_w(rw, wsrc, vo, lds, i + 2, lw); g2_issue_h(rw, hsrc, vo, lds, i + 3, lw);
    if (i == 5) { fy0 = flag_issue(p.ws + WS_FLAGY + cb8 * 512, 64, lane); fy1 = flag_issue(p.ws + WS_FLAGY + cb8 * 512 + 256, 16, lane); }
  }
  wait_vm_for<9>(fy0, fy1);
  if (!__all(fy0 == tag && fy1 == tag)) { poll_flags(p.ws + WS_FLAGY + cb8 * 512, 64, tag, lane, p.ws); poll_flags(p.ws + WS_FLAGY + cb8 * 512 + 256, 16, tag, lane, p.ws); }
  bar();
  g2_issue_w(rw, wsrc, vo, lds, 9, lw); g2_issue_h(rw, hsrc, vo, lds, 10, lw);
  {
    const int rl = lane >> 3, c = (lane & 7) ^ (((rl >> 1) & 3) << 1);
    const unsigned vy = rl * (S * 2) + c * 16, sy = WS_YT + cb8 * 80 * (S * 2) + row0 * 2;
#pragma unroll
    for (int j = 0; j < 3; ++j) { const int pc = min(lw * 3 + j, 9); bdma<SC1>(rw, lds + G2_YT + pc * 1024, vy, sy + pc * 8 * (S * 2)); }
  }
  wait_vm<16>(); bar();
  g2_issue_w(rw, wsrc, vo, lds, 10, lw); g2_issue_h(rw, hsrc, vo, lds, 11, lw);
  wait_vm<16>(); bar();
  g2_issue_w(rw, wsrc, vo, lds, 11, lw);
  wait_vm<9>(); bar();
  wait_vm<0>(); bar();
}

struct G2Frag { bf16x8 w[2][5], h[2]; };
template <int I, int HALF, int N> __device__ __forceinline__ void g2_read1(G2Frag& f, unsigned wa, unsigned ha) {
  constexpr int H = HALF * 2048, k = N / 6, j = N % 6;
  if constexpr (j == 0) rd128<H + k * 1024>(f.h[k], ha);
  else rd128<(2 * HALF + k) * 5120 + (j - 1) * 1024>(f.w[k][j - 1], wa);
}
template <int I, int HALF, int N> __device__ __forceinline__ void g2_step(const G2Frag& c, G2Frag& nx, f32x4 (&acc)[5], unsigned wa, unsigned ha, bool mul) {
  g2_read1<I, HALF, N>(nx, wa, ha);
  if constexpr (N < 10) { if (mul) acc[N % 5] = MFMA(c.w[N / 5][N % 5], c.h[N / 5], acc[N % 5]); }
  SCHED();
  if constexpr (N < 11) g2_step<I, HALF, N + 1>(c, nx, acc, wa, ha, mul);
  else if (mul) asm volatile("" ::"v"(c.h[0]), "v"(c.h[1]), "v"(c.w[0][0]), "v"(c.w[0][1]), "v"(c.w[0][2]), "v"(c.w[0][3]), "v"(c.w[0][4]),
                             "v"(c.w[1][0]), "v"(c.w[1][1]), "v"(c.w[1][2]), "v"(c.w[1][3]), "v"(c.w[1][4]));
}
__device__ __forceinline__ void g2_wait(G2Frag& f) {
  asm volatile("s_waitcnt lgkmcnt(0)" : "+v"(f.h[0]), "+v"(f.h[1]), "+v"(f.w[0][0]), "+v"(f.w[0][1]), "+v"(f.w[0][2]), "+v"(f.w[0][3]), "+v"(f.w[0][4]),
               "+v"(f.w[1][0]), "+v"(f.w[1][1]), "+v"(f.w[1][2]), "+v"(f.w[1][3]), "+v"(f.w[1][4])::"memory");
}
struct G2Epi { u32x2 qv[5]; float gt; unsigned fg; };
template <int I> __device__ __forceinline__ void g2_iter(const Params& p, G2Frag& X, G2Frag& Y, f32x4 (&acc)[5], unsigned wa, unsigned ha, char* yflag, unsigned tag, int lane,
                                                         G2Epi& e, int prow, int cb8) {
  bar();
  if constexpr (I == 1) {
    wait_vm_for<0>(e.fg);
    if (yflag && lane == 0) st_dev(yflag, tag);
    if (!__all(e.fg == tag)) poll_flags(p.ws + WS_FLAGG + ((prow - (lane & 15)) >> 5) * 4, 1, tag, lane, p.ws);
    e.gt = __uint_as_float(ld_dev(p.ws + WS_GATE + prow * 4));
#pragma unroll
    for (int ct = 0; ct < 5; ++ct) e.qv[ct] = *(const u32x2*)(p.q + (size_t)prow * D + cb8 * 80 + ct * 16 + 4 * (lane >> 4));
  }
  const unsigned w = wa + g2_wslot(I % 3), hh = ha + g2_hslot(I);
  g2_step<I, 0, 0>(Y, X, acc, w, hh, I > 0);
  g2_wait(X);
  g2_step<I, 1, 0>(X, Y, acc, w, hh, true);
  g2_wait(Y);
  if constexpr (I < 11) g2_iter<I + 1>(p, X, Y, acc, wa, ha, yflag, tag, lane, e, prow, cb8);
}

__device__ __forceinline__ void g2_consumer(const Params& p, LAS char* lds, int row0, int cb8, int rt, int lane, unsigned tag, char* yflag) {
  const int g = lane >> 4, j = lane & 15;
  const int prow = row0 + rt * 16 + j;
  G2Epi e;
  e.fg = flag_issue(p.ws + WS_FLAGG + ((row0 + rt * 16) >> 5) * 4, 1, lane);
  const unsigned lb = (unsigned)(size_t)lds;
  f32x4 acc[5];
#pragma unroll
  for (int ct = 0; ct < 5; ++ct) acc[ct] = f32x4{0, 0, 0, 0};
  G2Frag X, Y;
  g2_iter<0>(p, X, Y, acc, lb + lane * 16, lb + rt * 4096 + lane * 16, yflag, tag, lane, e, prow, cb8);
#pragma unroll
  for (int n = 0; n < 10; ++n) acc[n % 5] = MFMA(Y.w[n / 5][n % 5], Y.h[n / 5], acc[n % 5]);
  const float gt = e.gt;
  const u32x2* qv = e.qv;
  const int qq = j >> 2, pp = lane & 3;
  const float omg = 1.f - gt;
#pragma unroll
  for (int ct = 0; ct < 5; ++ct) {
    const int yr = 16 * ct + 4 * g + qq, yc = 2 * rt + (pp >> 1);
    const v4i16 yv = __builtin_amdgcn_ds_read_tr16_b64_v4i16((LAS v4i16*)(lds + G2_YT + yr * 128 + ((yc ^ (((yr >> 1) & 3) << 1)) << 4) + 8 * (pp & 1)));
    const u32x2 y2 = __builtin_bit_cast(u32x2, yv);
    const f32x4 b = acc[ct];
    u32x2 w;
    w[0] = pk(gt * (blo(qv[ct][0]) * blo(y2[0])) + omg * b[0], gt * (bhi(qv[ct][0]) * bhi(y2[0])) + omg * b[1]);
    w[1] = pk(gt * (blo(qv[ct][1]) * blo(y2[1])) + omg * b[2], gt * (bhi(qv[ct][1]) * bhi(y2[1])) + omg * b[3]);
    *(u32x2*)(p.out + (size_t)prow * D + cb8 * 80 + ct * 16 + 4 * g) = w;
  }
}

__global__ __launch_bounds__(NTHR, 2) void gm_kernel(Params p) {
  extern __shared__ __attribute__((aligned(16))) char shm[];
  LAS char* lds = (LAS char*)shm;
  const int tid = threadIdx.x, wid = __builtin_amdgcn_readfirstlane(tid >> 6), lane = tid & 63;
  const int b = blockIdx.x, xcd = b & 7, r = b >> 3;

  const bool is_g1 = r < 24;
  const int rg = r & 3, cs = r >> 2;
  const bool conv = r < 20;
  const int ch0 = xcd * 80 + r * 4;
  const int row0 = (xcd >> 2) * 1024 + rg * 256, cb = 6 * (xcd & 3) + cs;
  if (is_g1 && wid >= 4) {
    const G1Off off = g1_offsets(lane, wid - 4);
    g1_issue<8>(p, lds, 0, row0, cb, wid - 4, off); g1_issue<8>(p, lds + G1_SLOT, 1, row0, cb, wid - 4, off);
  }
  unsigned tag = 0;
  if (wid < 4) {
    unsigned epoch;
    asm volatile("global_load_dword %0, %1, off sc1\n\ts_waitcnt vmcnt(0)" : "=&v"(epoch) : "v"(p.ws + WS_EPOCH) : "memory");
    tag = __builtin_amdgcn_readfirstlane(epoch) + 1u;
    if (tid == 0) st_dev(p.ws + WS_ARRIVE + b * 4, tag);
    if (tid < 16) lds_signal(lds, tid, tid == 15 ? tag : tag - 1u);
    wait_lgkm0();
  }
  char* hflag = p.ws + WS_FLAGH + ((xcd >> 2) * 4 + rg) * 512 + (cb * 4 + wid) * 4;
  if (is_g1) {
    if (wid >= 4) g1_loader<8>(p, lds, row0, cb, conv, ch0, wid - 4, lane, tag);
    else {
      if (wid == 1) {
        unsigned long long pc; asm volatile("s_getpc_b64 %0" : "=s"(pc));
        const char* a = (const char*)(pc & ~127ull) + (r * 7 + min(lane, 6)) * 128;
        const char* t = lane < 40 ? p.ws + lane * (WS_END / 40) : (lane < 48 ? (const char*)p.out + (lane - 40) * (S * D * 2 / 8) : (lane < 56 ? (const char*)p.hk + (lane - 48) * (D * S * 2 / 8) : (const char*)p.wd + (lane - 56) * (F * D * 2 / 8)));
        __builtin_amdgcn_global_load_lds((const unsigned*)a, (LAS unsigned*)(lds + L_PF), 4, 0, 0);
        __builtin_amdgcn_global_load_lds((const unsigned*)t, (LAS unsigned*)(lds + L_PF + 256), 4, 0, SC1);
      }
      g1_consumer<8>(p, lds, row0, cb, wid, lane);
    }
  } else {
    const int sidx = xcd * 8 + (r - 24);
    bar();
    if (wid >= 4) tag = __builtin_amdgcn_readfirstlane(*(volatile LAS unsigned*)(lds + L_SIG + 60));
    if (wid < 5) {
      pm_issue(p, lds + 32768 + wid * PM_BYTES, sidx * 5 + wid, lane);
      pm_finish(p, lds + 32768 + wid * PM_BYTES, sidx * 5 + wid, lane, tag);
    } else if (wid >= 6) {
      __builtin_amdgcn_s_sleep(127);
      const int u0 = (sidx * 2 + (wid - 6)) * 3;
#pragma unroll
      for (int u = 0; u < 3; ++u) repack_issue(p, lds + ((wid - 6) * 3 + u) * 5120, (u0 + u) >> 3, (u0 + u) & 7, lane);
      wait_vm<0>();
#pragma unroll
      for (int u = 0; u < 3; ++u) repack_store(p, repack_addr(lds + ((wid - 6) * 3 + u) * 5120, (u0 + u) >> 3, (u0 + u) & 7, lane));
      wait_vm<0>();
      if (lane == 0) st_dev(p.ws + WS_FLAGW + (sidx * 2 + (wid - 6)) * 4, tag);
      aux_gate(p, xcd * 256 + (r - 24) * 32 + (wid - 6) * 16, lane);
      wait_vm<0>();
      if (wid == 7) lds_signal(lds, 9, tag);
      else { lds_await(lds, 9, tag); if (lane == 0) st_dev(p.ws + WS_FLAGG + sidx * 4, tag); }
    }
    if (b == 255 && wid == 1) {
      for (int i = 0; i < 4; ++i) poll_flags(p.ws + WS_ARRIVE + i * 256, 64, tag, lane, p.ws);
      if (lane == 0) st_dev(p.ws + WS_EPOCH, tag);
    }
  }
  const int cb8 = r >> 2, orow0 = xcd * 256 + (r & 3) * 64;
  if (wid >= 4) {
    wait_vm<0>();
    bar();
    const int lw = wid - 4;
    if (lw == 0) { poll_flags2(p.ws + WS_FLAGW, 64, p.ws + WS_FLAGW + 256, 64, tag, lane, p.ws); lds_signal(lds, 1, tag); }
    else lds_await(lds, 1, tag);
    const rsrc_t rw = mkrs(p.ws, WS_END);
#pragma unroll
    for (int st = 0; st < 2; ++st) g2_issue_w(rw, WS_WDP + cb8 * 48 * 5120, lane * 16, lds, st, lw);
    const char* w = p.ws + WS_WDP + (size_t)cb8 * 48 * 5120 + 2 * 20480 + ((r & 3) * 400 + lw * 100) * 128;
    unsigned d0, d1;
    asm volatile("global_load_dword %0, %1, off sc1" : "=v"(d0) : "v"(w + lane * 128) : "memory");
    asm volatile("global_load_dword %0, %1, off sc1" : "=v"(d1) : "v"(w + (64 + (lane < 36 ? lane : 35)) * 128) : "memory");
    if (lw == 0) {
      const char* f0 = p.ws + WS_FLAGH + (orow0 >> 8) * 512;
      poll_flags2(f0, 64, f0 + 256, 32, tag, lane, p.ws);
      lds_signal(lds, 2, tag);
    } else lds_await(lds, 2, tag);
    asm volatile("s_waitcnt vmcnt(0)" : "+v"(d0), "+v"(d1)::"memory");
    const unsigned hsrc = WS_H + ((orow0 >> 4) + lw) * 48 * 1024;
    g2_issue_h(rw, hsrc, lane * 16, lds, 0, lw); g2_issue_h(rw, hsrc, lane * 16, lds, 1, lw);
    if (conv) { lds_await(lds, 4, tag); lds_await(lds, 5, tag); lds_await(lds, 6, tag); lds_await(lds, 7, tag); }
    g2_issue_h(rw, hsrc, lane * 16, lds, 2, lw); g2_issue_h(rw, hsrc, lane * 16, lds, 3, lw);
  } else {
    if (is_g1) { wait_vm<0>(); if (lane == 0) st_dev(hflag, tag); }
    bar();
    if (conv) conv_channel(p, lds, ch0 + wid, wid, lane, tag);
  }
  bar();
  if (wid < 4) g2_consumer(p, lds, orow0, cb8, wid, lane, tag, conv ? p.ws + WS_FLAGY + ((ch0 + wid) / 80) * 512 + ((ch0 + wid) % 80) * 4 : nullptr);
  else g2_loader(p, lds, orow0, cb8, wid - 4, lane, tag);
}
}

extern "C" void kernel_launch(void* const* d_in, const int* in_sizes, int n_in, void* d_out, int out_size, void* d_ws, size_t ws_size, hipStream_t stream) {
  using namespace gm;
  Params p;
  p.q = (const u16*)d_in[0]; p.k = (const u16*)d_in[1]; p.v = (const u16*)d_in[2]; p.sk = (const u16*)d_in[3];
  p.hk = (const u16*)d_in[4]; p.dv = (const u16*)d_in[5]; p.wg = (const u16*)d_in[7]; p.wu = (const u16*)d_in[8];
  p.wd = (const u16*)d_in[9]; p.gw = (const u16*)d_in[10]; p.out = (u16*)d_out;
  p.ws = (char*)d_ws;
  (void)hipFuncSetAttribute((const void*)gm_kernel, hipFuncAttributeMaxDynamicSharedMemorySize, LDS_BYTES);
  gm_kernel<<<NWG, NTHR, LDS_BYTES, stream>>>(p);
}
```
